```python
import torch

def _softmax(x, axis=-1):
    return torch.softmax(x, dim=axis)
def _gelu(x):
    return torch.nn.functional.gelu(x, approximate="tanh")
def _silu(x):
    return torch.nn.functional.silu(x)
def _sigmoid(x):
    return torch.sigmoid(x)
def _layernorm(x, axis=-1, eps=1e-5):
    m = x.mean(dim=axis, keepdim=True)
    v = x.var(dim=axis, keepdim=True, unbiased=False)
    return (x - m) / torch.sqrt(v + eps)
def _scatter_add(tgt, idx, src, axis=0):
    return tgt.clone().index_add_(axis, idx, src)
def _segment_sum(src, seg, n):
    out = torch.zeros((n,) + src.shape[1:], dtype=src.dtype)
    return out.index_add_(0, seg, src)
def _pad(x, pw):
    flat = tuple(v for pair in reversed(pw) for v in pair)
    return torch.nn.functional.pad(x, flat)

kernel_name = 'concat_heads__gqa_block_cached__gqa_block_cached__near_ridge'

def setup_inputs():

    _w = (lambda a: (lambda t: t.to(torch.bfloat16) if t.is_floating_point() else t)(torch.as_tensor(a)))
    def _A_setup():
        B, Sq, D = 8, 64, 448
        Hq, Hkv, Sk, Dh = 16, 8, 512, 256
        assert Hq % Hkv == 0, (Hq, Hkv)
        return {
            "x": _w((lambda *s: torch.randn(*s, dtype=torch.bfloat16) * 0.8)(B, Sq, D)),
            "k_cache": _w((lambda *s: torch.randn(*s, dtype=torch.bfloat16) * 0.8)(B, Hkv, Sk, Dh)),
            "v_cache": _w((lambda *s: torch.randn(*s, dtype=torch.bfloat16) * 0.05)(B, Hkv, Sk, Dh)),
            "wq": _w((lambda *s: torch.randn(*s, dtype=torch.bfloat16) * 0.8)(D, Hq * Dh) * (D ** -0.5)),
            "wo": _w((lambda *s: torch.randn(*s, dtype=torch.bfloat16) * 0.05)(Hq * Dh, D) * ((Hq * Dh) ** -0.5)),
        }
    _A_in = _A_setup()

    def _B_setup():
        B, Sq, D = 8, 64, 448
        Hq, Hkv, Sk, Dh = 16, 8, 512, 256
        assert Hq % Hkv == 0, (Hq, Hkv)
        return {
            "x": _w((lambda *s: torch.randn(*s, dtype=torch.bfloat16) * 0.8)(B, Sq, D)),
            "k_cache": _w((lambda *s: torch.randn(*s, dtype=torch.bfloat16) * 0.8)(B, Hkv, Sk, Dh)),
            "v_cache": _w((lambda *s: torch.randn(*s, dtype=torch.bfloat16) * 0.05)(B, Hkv, Sk, Dh)),
            "wq": _w((lambda *s: torch.randn(*s, dtype=torch.bfloat16) * 0.8)(D, Hq * Dh) * (D ** -0.5)),
            "wo": _w((lambda *s: torch.randn(*s, dtype=torch.bfloat16) * 0.05)(Hq * Dh, D) * ((Hq * Dh) ** -0.5)),
        }
    _B_in = _B_setup()

    return {
        "A_x": _A_in["x"],
        "A_k_cache": _A_in["k_cache"],
        "A_v_cache": _A_in["v_cache"],
        "A_wq": _A_in["wq"],
        "A_wo": _A_in["wo"],
        "B_x": _B_in["x"],
        "B_k_cache": _B_in["k_cache"],
        "B_v_cache": _B_in["v_cache"],
        "B_wq": _B_in["wq"],
        "B_wo": _B_in["wo"],
    }

def reference(A_x, A_k_cache, A_v_cache, A_wq, A_wo, B_x, B_k_cache, B_v_cache, B_wq, B_wo):
    def _A_ref(x, k_cache, v_cache, wq, wo):
        B, Sq, D = x.shape
        Hq, Dh = 16, 256
        Hkv = k_cache.shape[1]
        rep = Hq // Hkv
        q = (lambda x, *d: x.permute(*d))((x @ wq).reshape(B, Sq, Hq, Dh), 0, 2, 1, 3)
        k = (lambda x, n, axis: torch.repeat_interleave(x, n, dim=axis))(k_cache, rep, axis=1)
        v = (lambda x, n, axis: torch.repeat_interleave(x, n, dim=axis))(v_cache, rep, axis=1)
        scores = torch.einsum("bhqd,bhkd->bhqk", q, k) * (Dh ** -0.5)
        p = _softmax(scores, axis=-1)
        o = torch.einsum("bhqk,bhkd->bhqd", p, v)
        return (lambda x, *d: x.permute(*d))(o, 0, 2, 1, 3).reshape(B, Sq, Hq * Dh) @ wo

    def _B_ref(x, k_cache, v_cache, wq, wo):
        B, Sq, D = x.shape
        Hq, Dh = 16, 256
        Hkv = k_cache.shape[1]
        rep = Hq // Hkv
        q = (lambda x, *d: x.permute(*d))((x @ wq).reshape(B, Sq, Hq, Dh), 0, 2, 1, 3)
        k = (lambda x, n, axis: torch.repeat_interleave(x, n, dim=axis))(k_cache, rep, axis=1)
        v = (lambda x, n, axis: torch.repeat_interleave(x, n, dim=axis))(v_cache, rep, axis=1)
        scores = torch.einsum("bhqd,bhkd->bhqk", q, k) * (Dh ** -0.5)
        p = _softmax(scores, axis=-1)
        o = torch.einsum("bhqk,bhkd->bhqd", p, v)
        return (lambda x, *d: x.permute(*d))(o, 0, 2, 1, 3).reshape(B, Sq, Hq * Dh) @ wo

    a = _A_ref(A_x, A_k_cache, A_v_cache, A_wq, A_wo)
    b = _B_ref(B_x, B_k_cache, B_v_cache, B_wq, B_wo)
    return torch.concatenate([a, b], axis=-1)
```

```cpp
#include <hip/hip_runtime.h>
#include <hip/hip_bf16.h>

typedef __attribute__((ext_vector_type(8))) __bf16 bf16x8;
typedef __attribute__((ext_vector_type(4))) __bf16 bf16x4;
typedef __attribute__((ext_vector_type(2))) __bf16 bf16x2;
typedef __attribute__((ext_vector_type(16))) float f32x16;
typedef __attribute__((ext_vector_type(4))) float f32x4;
typedef __attribute__((ext_vector_type(2))) float f32x2;
typedef __attribute__((ext_vector_type(4))) unsigned u32x4;
#define LDS_AS __attribute__((address_space(3)))
#define DI __device__ __forceinline__

#define NT_KV 1

constexpr int kD = 448, kHq = 16, kHkv = 8, kSk = 512, kDh = 256, kSq = 64;
constexpr unsigned kRegionA = 0;
constexpr unsigned kRing = 65536;
constexpr unsigned kLdsBytes = 163840;
constexpr int kPartBytes = 7 * 8192;

#define BAR() asm volatile("s_waitcnt lgkmcnt(0)\n\ts_barrier" ::: "memory")
#define LGKM0() asm volatile("s_waitcnt lgkmcnt(0)" ::: "memory")
#define VMW(n) asm volatile("s_waitcnt vmcnt(" #n ")" ::: "memory")

template <bool NT>
DI void dma4(const char* sbase, unsigned ldsdst, unsigned v0, unsigned v1, unsigned v2, unsigned v3) {
  unsigned keep;
  if (NT) {
    asm volatile(
        "s_mov_b32 %0, m0\n\ts_mov_b32 m0, %6\n\ts_nop 0\n\t"
        "global_load_lds_dwordx4 %1, %5 nt\n\ts_add_u32 m0, m0, 0x400\n\ts_nop 0\n\t"
        "global_load_lds_dwordx4 %2, %5 nt\n\ts_add_u32 m0, m0, 0x400\n\ts_nop 0\n\t"
        "global_load_lds_dwordx4 %3, %5 nt\n\ts_add_u32 m0, m0, 0x400\n\ts_nop 0\n\t"
        "global_load_lds_dwordx4 %4, %5 nt\n\ts_mov_b32 m0, %0"
        : "=&s"(keep)
        : "v"(v0), "v"(v1), "v"(v2), "v"(v3), "s"(sbase), "s"(ldsdst)
        : "memory", "scc");
  } else {
    asm volatile(
        "s_mov_b32 %0, m0\n\ts_mov_b32 m0, %6\n\ts_nop 0\n\t"
        "global_load_lds_dwordx4 %1, %5\n\ts_add_u32 m0, m0, 0x400\n\ts_nop 0\n\t"
        "global_load_lds_dwordx4 %2, %5\n\ts_add_u32 m0, m0, 0x400\n\ts_nop 0\n\t"
        "global_load_lds_dwordx4 %3, %5\n\ts_add_u32 m0, m0, 0x400\n\ts_nop 0\n\t"
        "global_load_lds_dwordx4 %4, %5\n\ts_mov_b32 m0, %0"
        : "=&s"(keep)
        : "v"(v0), "v"(v1), "v"(v2), "v"(v3), "s"(sbase), "s"(ldsdst)
        : "memory", "scc");
  }
}

DI void dma1(const char* sbase, unsigned ldsdst, unsigned v0) {
  unsigned keep;
  asm volatile("s_mov_b32 %0, m0\n\ts_mov_b32 m0, %3\n\ts_nop 0\n\tglobal_load_lds_dwordx4 %1, %2\n\ts_mov_b32 m0, %0"
               : "=&s"(keep)
               : "v"(v0), "s"(sbase), "s"(ldsdst)
               : "memory");
}

DI bf16x8 lds_b128(unsigned a) { return *(const LDS_AS bf16x8*)(size_t)a; }
DI f32x4 lds_f4(unsigned a) { return *(const LDS_AS f32x4*)(size_t)a; }
DI float lds_f1(unsigned a) { return *(const LDS_AS float*)(size_t)a; }
DI void sts_b128(unsigned a, bf16x8 v) { *(LDS_AS bf16x8*)(size_t)a = v; }
DI void sts_f4(unsigned a, f32x4 v) { *(LDS_AS f32x4*)(size_t)a = v; }
DI void sts_f1(unsigned a, float v) { *(LDS_AS float*)(size_t)a = v; }
DI bf16x4 lds_tr(unsigned a) { return __builtin_amdgcn_ds_read_tr16_b64_v4bf16((LDS_AS bf16x4*)(size_t)a); }
DI bf16x8 cat(bf16x4 a, bf16x4 b) { return __builtin_shufflevector(a, b, 0, 1, 2, 3, 4, 5, 6, 7); }
DI f32x16 mfma(bf16x8 a, bf16x8 b, f32x16 c) { return __builtin_amdgcn_mfma_f32_32x32x16_bf16(a, b, c, 0, 0, 0); }

DI bf16x8 pack8(const f32x16& t, int s, float scale) {
  bf16x8 r;
#pragma unroll
  for (int j = 0; j < 4; ++j) {
    f32x2 p = {t[8 * s + 2 * j] * scale, t[8 * s + 2 * j + 1] * scale};
    bf16x2 c = __builtin_convertvector(p, bf16x2);
    r[2 * j] = c[0];
    r[2 * j + 1] = c[1];
  }
  return r;
}
DI bf16x8 pack8(const f32x16& t, int s) {
  bf16x8 r;
#pragma unroll
  for (int j = 0; j < 4; ++j) {
    f32x2 p = {t[8 * s + 2 * j], t[8 * s + 2 * j + 1]};
    bf16x2 c = __builtin_convertvector(p, bf16x2);
    r[2 * j] = c[0];
    r[2 * j + 1] = c[1];
  }
  return r;
}
DI float other_half(float v) { return __shfl_xor(v, 32, 64); }

struct Ctx {
  const char *wq, *kc, *vc, *wo;
  unsigned ring;
  int w, ds, kh, b, lane;
};

template <int U>
DI void issue(const Ctx& c) {
  const unsigned l = c.lane, r = l >> 3, ch = l & 7;
  const unsigned dst = c.ring + (U % 3) * 4096;
  const unsigned ctr = 16 * (ch ^ (4 * ((l >> 4) & 1)));
  if constexpr (U < 7) {
    int uu = U + (c.b == 7 ? 0 : c.b);
    if (uu >= 7) uu -= 7;
    const unsigned v = r * 8192 + ctr;
    dma4<false>(c.wq + (size_t)uu * (32 * 8192), dst, v, v + 8 * 8192, v + 16 * 8192, v + 24 * 8192);
  } else if constexpr (U < 15) {
    constexpr int uu = U - 7, dc = uu >> 1, kb = uu & 1;
    const unsigned ve = r * 512 + 16 * (ch ^ (l >> 4)), vo = r * 512 + 16 * (ch ^ (4 + (l >> 4)));
    dma4<NT_KV>(c.kc + kb * (32 * 512) + dc * 128, dst, ve, vo + 8 * 512, ve + 16 * 512, vo + 24 * 512);
  } else if constexpr (U < 23) {
    constexpr int uu = U - 15;
    const unsigned v = r * 512 + ctr;
    dma4<NT_KV>(c.vc + uu * (32 * 512), dst, v, v + 8 * 512, v + 16 * 512, v + 24 * 512);
  } else if constexpr (U < 31) {
    if (c.w < 7) {
      const int uu = (U - 23 + c.b) & 7;
      const unsigned v = r * 896 + ctr;
      dma4<false>(c.wo + uu * (32 * 896), dst, v, v + 8 * 896, v + 16 * 896, v + 24 * 896);
    }
  }
}

template <int U>
DI void wait_unit(const Ctx& c) {
  if constexpr (U == 21 || U == 22) {
    if (c.w == 7) VMW(0);
  }
  if constexpr (U <= 28) VMW(8);
  else if constexpr (U == 29) VMW(4);
  else VMW(0);
}

template <int U, int TOFF>
DI void tr_unit(const Ctx& c, unsigned abase, const bf16x8 (&bf)[2][2], f32x16 (&acc)[2][2]) {
  wait_unit<U>(c);
  bf16x8 af[2][2];
  const unsigned a0 = abase + (U % 3) * 4096;
#pragma unroll
  for (int s = 0; s < 2; ++s)
#pragma unroll
    for (int cb = 0; cb < 2; ++cb) {
      const unsigned a = (cb ? (a0 ^ 64u) : a0) + s * 2048;
      af[s][cb] = cat(lds_tr(a), lds_tr(a + TOFF));
    }
  LGKM0();
  issue<U + 3>(c);
#pragma unroll
  for (int s = 0; s < 2; ++s)
#pragma unroll
    for (int cb = 0; cb < 2; ++cb)
#pragma unroll
      for (int qb = 0; qb < 2; ++qb) acc[cb][qb] = mfma(af[s][cb], bf[s][qb], acc[cb][qb]);
}

DI void zero_acc(f32x16 (&acc)[2][2]) {
#pragma unroll
  for (int i = 0; i < 2; ++i)
#pragma unroll
    for (int j = 0; j < 2; ++j)
#pragma unroll
      for (int r = 0; r < 16; ++r) acc[i][j][r] = 0.f;
}

DI void exch_send(const Ctx& c, const f32x16 (&acc)[2][2]) {
  const unsigned dst = kRegionA + (unsigned)(c.w ^ 4) * 8192 + c.lane * 16;
#pragma unroll
  for (int db = 0; db < 2; ++db)
#pragma unroll
    for (int r4 = 0; r4 < 4; ++r4) {
      f32x4 v;
#pragma unroll
      for (int j = 0; j < 4; ++j) v[j] = c.kh ? acc[db][0][4 * r4 + j] : acc[db][1][4 * r4 + j];
      sts_f4(dst + (db * 4 + r4) * 1024, v);
    }
}
DI void exch_recv(const Ctx& c, const f32x16 (&acc)[2][2], f32x16 (&t)[2]) {
  const unsigned src = kRegionA + (unsigned)c.w * 8192 + c.lane * 16;
#pragma unroll
  for (int db = 0; db < 2; ++db)
#pragma unroll
    for (int r4 = 0; r4 < 4; ++r4) {
      const f32x4 v = lds_f4(src + (db * 4 + r4) * 1024);
#pragma unroll
      for (int j = 0; j < 4; ++j) t[db][4 * r4 + j] = v[j] + (c.kh ? acc[db][1][4 * r4 + j] : acc[db][0][4 * r4 + j]);
    }
}

template <int U>
DI void q_units(const Ctx& c, unsigned abase, unsigned xrow, unsigned xsw, f32x16 (&acc)[2][2]) {
  if constexpr (U < 7) {
    int uu = U + (c.b == 7 ? 0 : c.b);
    if (uu >= 7) uu -= 7;
    const unsigned ks0 = 14 * c.kh + 2 * uu;
    bf16x8 bf[2][2];
#pragma unroll
    for (int s = 0; s < 2; ++s) {
      const unsigned ks = ks0 + s;
      const unsigned a = kRegionA + (ks >> 2) * 8192 + xrow + (((ks & 3) * 32) ^ xsw);
      bf[s][0] = lds_b128(a);
      bf[s][1] = lds_b128(a + 4096);
    }
    tr_unit<U, 512>(c, abase, bf, acc);
    q_units<U + 1>(c, abase, xrow, xsw, acc);
  }
}

template <int U>
DI void s_units(const Ctx& c, unsigned xrow, unsigned xsw, bf16x8 (&qf)[4][2], f32x16 (&acc)[2][2]) {
  if constexpr (U < 15) {
    constexpr int uu = U - 7, dc = uu >> 1, kb = uu & 1;
    if constexpr (kb == 0) {
#pragma unroll
      for (int s = 0; s < 4; ++s)
#pragma unroll
        for (int qb = 0; qb < 2; ++qb) qf[s][qb] = lds_b128(kRegionA + (dc + 4 * qb) * 8192 + s * 1024 + c.lane * 16);
    }
    wait_unit<U>(c);
    bf16x8 af[4];
#pragma unroll
    for (int s = 0; s < 4; ++s) af[s] = lds_b128(c.ring + (U % 3) * 4096 + xrow + ((s * 32) ^ xsw));
    LGKM0();
    issue<U + 3>(c);
#pragma unroll
    for (int s = 0; s < 4; ++s)
#pragma unroll
      for (int qb = 0; qb < 2; ++qb) acc[kb][qb] = mfma(af[s], qf[s][qb], acc[kb][qb]);
    s_units<U + 1>(c, xrow, xsw, qf, acc);
  }
}

template <int U>
DI void v_units(const Ctx& c, unsigned abase, f32x16 (&acc)[2][2]) {
  if constexpr (U < 23) {
    constexpr int uu = U - 15;
    bf16x8 bf[2][2];
    const unsigned pb = kRegionA + (unsigned)c.kh * 32768 + uu * 4096 + c.lane * 16;
#pragma unroll
    for (int s = 0; s < 2; ++s)
#pragma unroll
      for (int qb = 0; qb < 2; ++qb) bf[s][qb] = lds_b128(pb + s * 2048 + qb * 1024);
    tr_unit<U, 1024>(c, abase, bf, acc);
    v_units<U + 1>(c, abase, acc);
  }
}

template <int U>
DI void o_units(const Ctx& c, unsigned abase, f32x16 (&acc)[2][2]) {
  if constexpr (U < 31) {
    const unsigned uu = (U - 23 + c.b) & 7;
    bf16x8 bf[2][2];
    const unsigned ob = kRegionA + (uu >> 1) * 8192 + (uu & 1) * 2048 + c.lane * 16;
#pragma unroll
    for (int s = 0; s < 2; ++s)
#pragma unroll
      for (int qb = 0; qb < 2; ++qb) bf[s][qb] = lds_b128(ob + s * 1024 + qb * 32768);
    tr_unit<U, 1024>(c, abase, bf, acc);
    o_units<U + 1>(c, abase, acc);
  }
}

__global__ __launch_bounds__(512, 2) void gqa_fused_kernel(
    const char* __restrict__ ax, const char* __restrict__ ak, const char* __restrict__ av, const char* __restrict__ awq,
    const char* __restrict__ awo, const char* __restrict__ bx, const char* __restrict__ bk, const char* __restrict__ bv,
    const char* __restrict__ bwq, const char* __restrict__ bwo, char* __restrict__ part) {
  const int bid = blockIdx.x, xcd = bid & 7, idx = bid >> 3;
  const int br = xcd >> 2, hq = (xcd & 3) * 4 + (idx & 3), b = idx >> 2, hkv = hq >> 1;
  const int lane = threadIdx.x & 63;
  const int w = __builtin_amdgcn_readfirstlane(threadIdx.x >> 6);
  const int ds = w & 3, kh = w >> 2;
  const unsigned l = lane, h = l >> 5;

  Ctx c;
  c.w = w; c.ds = ds; c.kh = kh; c.b = b; c.lane = lane;
  c.ring = kRing + (unsigned)w * 12288;
  c.wq = (br ? bwq : awq) + ((size_t)(224 * kh) * 4096 + hq * 256 + 64 * ds) * 2;
  c.kc = (br ? bk : ak) + ((size_t)((b * kHkv + hkv) * kSk + 64 * w) * kDh) * 2;
  c.vc = (br ? bv : av) + ((size_t)((b * kHkv + hkv) * kSk + 256 * kh) * kDh + 64 * ds) * 2;
  c.wo = (br ? bwo : awo) + ((size_t)(hq * 256) * kD + 64 * w) * 2;

  {
    const char* xs = (br ? bx : ax) + (size_t)(b * kSq + 8 * w) * (kD * 2);
    const unsigned row = 8 * w + (l >> 3);
    const unsigned v = (l >> 3) * 896 + 16 * ((l & 7) ^ ((row >> 1) & 7));
#pragma unroll
    for (int j = 0; j < 7; ++j) dma1(xs + j * 128, kRegionA + j * 8192 + w * 1024, v);
  }
  issue<0>(c);
  issue<1>(c);
  issue<2>(c);

  const unsigned blk = (l >> 4) & 1, q4 = (l & 15) >> 2, p = l & 3, pp = ((p & 1) << 1) | (p >> 1);
  const unsigned trc = c.ring + 32 * blk + 64 * ((q4 >> 1) & 1);
  const unsigned xrow = (l & 31) * 128, xsw = 16 * (h ^ (((l & 31) >> 1) & 7));

  f32x16 acc[2][2];
  f32x16 t[2];
  zero_acc(acc);
  VMW(12);
  BAR();

  q_units<0>(c, trc + 128 * (8 * h + q4) + 8 * pp, xrow, xsw, acc);
  BAR();
  exch_send(c, acc);
  BAR();
  exch_recv(c, acc, t);
  LGKM0();
#pragma unroll
  for (int db = 0; db < 2; ++db)
#pragma unroll
    for (int s = 0; s < 2; ++s) sts_b128(kRegionA + w * 8192 + (2 * db + s) * 1024 + l * 16, pack8(t[db], s));
  zero_acc(acc);
  BAR();

  {
    bf16x8 qf[4][2];
    s_units<7>(c, xrow, xsw, qf, acc);
  }

  float lsum[2];
  {
    float mx[2];
#pragma unroll
    for (int qb = 0; qb < 2; ++qb) {
      float m = acc[0][qb][0];
#pragma unroll
      for (int kb = 0; kb < 2; ++kb)
#pragma unroll
        for (int r = 0; r < 16; ++r) m = fmaxf(m, acc[kb][qb][r]);
      mx[qb] = fmaxf(m, other_half(m));
    }
    sts_f1(kRegionA + w * 8192 + 4096 + l * 4, h ? mx[1] : mx[0]);
    BAR();
    float m = lds_f1(kRegionA + 4096 + l * 4);
#pragma unroll
    for (int i = 1; i < 8; ++i) m = fmaxf(m, lds_f1(kRegionA + i * 8192 + 4096 + l * 4));
    const float mo = other_half(m);
    const float sc = 0.0625f * 1.44269504088896340736f;
    const float m0 = (h ? mo : m) * sc, m1 = (h ? m : mo) * sc;
#pragma unroll
    for (int qb = 0; qb < 2; ++qb) {
      float sum = 0.f;
      const float mm = qb ? m1 : m0;
#pragma unroll
      for (int kb = 0; kb < 2; ++kb)
#pragma unroll
        for (int r = 0; r < 16; ++r) {
          const float e = __builtin_amdgcn_exp2f(fmaf(acc[kb][qb][r], sc, -mm));
          acc[kb][qb][r] = e;
          sum += e;
        }
      lsum[qb] = sum + other_half(sum);
    }
    BAR();
#pragma unroll
    for (int kb = 0; kb < 2; ++kb)
#pragma unroll
      for (int s = 0; s < 2; ++s)
#pragma unroll
        for (int qb = 0; qb < 2; ++qb)
          sts_b128(kRegionA + w * 8192 + ((2 * kb + s) * 2 + qb) * 1024 + l * 16, pack8(acc[kb][qb], s));
    zero_acc(acc);
    BAR();
  }

  v_units<15>(c, trc + 128 * (4 * h + q4) + 8 * p, acc);
  BAR();
  exch_send(c, acc);
  constexpr unsigned kStat = kRing + 7 * 12288;
  sts_f1(kStat + w * 256 + l * 4, h ? lsum[1] : lsum[0]);
  BAR();
  exch_recv(c, acc, t);
  {
    float tot = 0.f;
#pragma unroll
    for (int i = 0; i < 8; ++i) tot += lds_f1(kStat + i * 256 + (32 * kh + (l & 31)) * 4);
    const float inv = 1.0f / tot;
    LGKM0();
#pragma unroll
    for (int db = 0; db < 2; ++db)
#pragma unroll
      for (int s = 0; s < 2; ++s) sts_b128(kRegionA + w * 8192 + (2 * db + s) * 1024 + l * 16, pack8(t[db], s, inv));
  }
  zero_acc(acc);
  BAR();

  if (w < 7) {
    o_units<23>(c, trc + 128 * (4 * h + q4) + 8 * pp, acc);
    char* dst = part + (size_t)((br * 8 + b) * kHq + hq) * kPartBytes + w * 8192 + l * 16;
#pragma unroll
    for (int nb = 0; nb < 2; ++nb)
#pragma unroll
      for (int qb = 0; qb < 2; ++qb)
#pragma unroll
        for (int s = 0; s < 2; ++s) *(bf16x8*)(dst + ((nb * 2 + qb) * 2 + s) * 1024) = pack8(acc[nb][qb], s);
  }
}

__global__ __launch_bounds__(256) void head_reduce_kernel(const char* __restrict__ part, char* __restrict__ out) {
  const int t = blockIdx.x * 256 + threadIdx.x;
  const int g = t / 3584, ck = t % 3584;
  const char* src = part + (size_t)g * kHq * kPartBytes + ck * 16;
  u32x4 v[16];
#pragma unroll
  for (int i = 0; i < 16; ++i) v[i] = *(const u32x4*)(src + (size_t)i * kPartBytes);
  float s[8];
#pragma unroll
  for (int j = 0; j < 8; ++j) s[j] = 0.f;
#pragma unroll
  for (int i = 0; i < 16; ++i)
#pragma unroll
    for (int j = 0; j < 4; ++j) {
      s[2 * j] += __builtin_bit_cast(float, v[i][j] << 16);
      s[2 * j + 1] += __builtin_bit_cast(float, v[i][j] & 0xffff0000u);
    }
  bf16x8 r;
#pragma unroll
  for (int j = 0; j < 4; ++j) {
    f32x2 pr = {s[2 * j], s[2 * j + 1]};
    bf16x2 cv = __builtin_convertvector(pr, bf16x2);
    r[2 * j] = cv[0];
    r[2 * j + 1] = cv[1];
  }
  const int lane = ck & 63, fr = (ck >> 6) & 7, w = ck >> 9;
  const int nb = fr >> 2, qb = (fr >> 1) & 1, sh = fr & 1;
  const int q = 32 * qb + (lane & 31), n = 64 * w + 32 * nb + 16 * sh + 8 * (lane >> 5);
  const int br = g >> 3, b = g & 7;
  *(bf16x8*)(out + ((size_t)(b * kSq + q) * (2 * kD) + br * kD + n) * 2) = r;
}

extern "C" void kernel_launch(void* const* d_in, const int* in_sizes, int n_in, void* d_out, int out_size, void* d_ws,
                              size_t ws_size, hipStream_t stream) {
  (void)in_sizes; (void)n_in; (void)out_size; (void)ws_size;
  (void)hipFuncSetAttribute((const void*)gqa_fused_kernel, hipFuncAttributeMaxDynamicSharedMemorySize, kLdsBytes);
  gqa_fused_kernel<<<256, 512, kLdsBytes, stream>>>(
      (const char*)d_in[0], (const char*)d_in[1], (const char*)d_in[2], (const char*)d_in[3], (const char*)d_in[4],
      (const char*)d_in[5], (const char*)d_in[6], (const char*)d_in[7], (const char*)d_in[8], (const char*)d_in[9],
      (char*)d_ws);
  head_reduce_kernel<<<224, 256, 0, stream>>>((const char*)d_ws, (char*)d_out);
}
```

```cpp
#include <hip/hip_runtime.h>
#include <hip/hip_bf16.h>

typedef __attribute__((ext_vector_type(8))) __bf16 bf16x8;
typedef __attribute__((ext_vector_type(4))) __bf16 bf16x4;
typedef __attribute__((ext_vector_type(2))) __bf16 bf16x2;
typedef __attribute__((ext_vector_type(16))) float f32x16;
typedef __attribute__((ext_vector_type(4))) float f32x4;
typedef __attribute__((ext_vector_type(2))) float f32x2;
typedef __attribute__((ext_vector_type(4))) unsigned u32x4;
#define LDS_AS __attribute__((address_space(3)))
#define DI __device__ __forceinline__

#define NT_KV 0

constexpr int kD = 448, kHq = 16, kHkv = 8, kSk = 512, kDh = 256, kSq = 64;
constexpr unsigned kRegionA = 0;
constexpr unsigned kRing = 65536;
constexpr unsigned kLdsBytes = 163840;
constexpr int kPartBytes = 7 * 8192;

#define BAR() asm volatile("s_waitcnt lgkmcnt(0)\n\ts_barrier" ::: "memory")
#define LGKM0() asm volatile("s_waitcnt lgkmcnt(0)" ::: "memory")
#define VMW(n) asm volatile("s_waitcnt vmcnt(" #n ")" ::: "memory")
#define STAMP(i)

template <bool NT>
DI void dma4(const char* sbase, unsigned ldsdst, unsigned v0, unsigned v1, unsigned v2, unsigned v3) {
  unsigned keep;
  if (NT) {
    asm volatile(
        "s_mov_b32 %0, m0\n\ts_mov_b32 m0, %6\n\ts_nop 0\n\t"
        "global_load_lds_dwordx4 %1, %5 nt\n\ts_add_u32 m0, m0, 0x400\n\ts_nop 0\n\t"
        "global_load_lds_dwordx4 %2, %5 nt\n\ts_add_u32 m0, m0, 0x400\n\ts_nop 0\n\t"
        "global_load_lds_dwordx4 %3, %5 nt\n\ts_add_u32 m0, m0, 0x400\n\ts_nop 0\n\t"
        "global_load_lds_dwordx4 %4, %5 nt\n\ts_mov_b32 m0, %0"
        : "=&s"(keep)
        : "v"(v0), "v"(v1), "v"(v2), "v"(v3), "s"(sbase), "s"(ldsdst)
        : "memory", "scc");
  } else {
    asm volatile(
        "s_mov_b32 %0, m0\n\ts_mov_b32 m0, %6\n\ts_nop 0\n\t"
        "global_load_lds_dwordx4 %1, %5\n\ts_add_u32 m0, m0, 0x400\n\ts_nop 0\n\t"
        "global_load_lds_dwordx4 %2, %5\n\ts_add_u32 m0, m0, 0x400\n\ts_nop 0\n\t"
        "global_load_lds_dwordx4 %3, %5\n\ts_add_u32 m0, m0, 0x400\n\ts_nop 0\n\t"
        "global_load_lds_dwordx4 %4, %5\n\ts_mov_b32 m0, %0"
        : "=&s"(keep)
        : "v"(v0), "v"(v1), "v"(v2), "v"(v3), "s"(sbase), "s"(ldsdst)
        : "memory", "scc");
  }
}

DI void dma1(const char* sbase, unsigned ldsdst, unsigned v0) {
  unsigned keep;
  asm volatile("s_mov_b32 %0, m0\n\ts_mov_b32 m0, %3\n\ts_nop 0\n\tglobal_load_lds_dwordx4 %1, %2\n\ts_mov_b32 m0, %0"
               : "=&s"(keep)
               : "v"(v0), "s"(sbase), "s"(ldsdst)
               : "memory");
}

DI bf16x8 lds_b128(unsigned a) { return *(const LDS_AS bf16x8*)(size_t)a; }
DI f32x4 lds_f4(unsigned a) { return *(const LDS_AS f32x4*)(size_t)a; }
DI float lds_f1(unsigned a) { return *(const LDS_AS float*)(size_t)a; }
DI void sts_b128(unsigned a, bf16x8 v) { *(LDS_AS bf16x8*)(size_t)a = v; }
DI void sts_f4(unsigned a, f32x4 v) { *(LDS_AS f32x4*)(size_t)a = v; }
DI void sts_f1(unsigned a, float v) { *(LDS_AS float*)(size_t)a = v; }
DI bf16x4 lds_tr(unsigned a) { return __builtin_amdgcn_ds_read_tr16_b64_v4bf16((LDS_AS bf16x4*)(size_t)a); }
DI bf16x8 cat(bf16x4 a, bf16x4 b) { return __builtin_shufflevector(a, b, 0, 1, 2, 3, 4, 5, 6, 7); }
DI f32x16 mfma(bf16x8 a, bf16x8 b, f32x16 c) { return __builtin_amdgcn_mfma_f32_32x32x16_bf16(a, b, c, 0, 0, 0); }

DI bf16x8 pack8(const f32x16& t, int s, float scale) {
  bf16x8 r;
#pragma unroll
  for (int j = 0; j < 4; ++j) {
    f32x2 p = {t[8 * s + 2 * j] * scale, t[8 * s + 2 * j + 1] * scale};
    bf16x2 c = __builtin_convertvector(p, bf16x2);
    r[2 * j] = c[0];
    r[2 * j + 1] = c[1];
  }
  return r;
}
DI bf16x8 pack8(const f32x16& t, int s) {
  bf16x8 r;
#pragma unroll
  for (int j = 0; j < 4; ++j) {
    f32x2 p = {t[8 * s + 2 * j], t[8 * s + 2 * j + 1]};
    bf16x2 c = __builtin_convertvector(p, bf16x2);
    r[2 * j] = c[0];
    r[2 * j + 1] = c[1];
  }
  return r;
}
DI float other_half(float v) { return __shfl_xor(v, 32, 64); }

struct Ctx {
  const char *wq, *kc, *vc, *wo;
  unsigned ring;
  int w, ds, kh, b, lane;
};

template <int U>
DI void issue(const Ctx& c) {
  const unsigned l = c.lane, r = l >> 3, ch = l & 7;
  const unsigned dst = c.ring + (U % 3) * 4096;
  const unsigned ctr = 16 * (ch ^ (4 * ((l >> 4) & 1)));
  if constexpr (U < 7) {
    int uu = U + (c.b == 7 ? 0 : c.b);
    if (uu >= 7) uu -= 7;
    const unsigned v = r * 8192 + ctr;
    dma4<false>(c.wq + (size_t)uu * (32 * 8192), dst, v, v + 8 * 8192, v + 16 * 8192, v + 24 * 8192);
  } else if constexpr (U < 15) {
    constexpr int uu = U - 7, dc = uu >> 1, kb = uu & 1;
    const unsigned ve = r * 512 + 16 * (ch ^ (l >> 4)), vo = r * 512 + 16 * (ch ^ (4 + (l >> 4)));
    dma4<NT_KV>(c.kc + kb * (32 * 512) + dc * 128, dst, ve, vo + 8 * 512, ve + 16 * 512, vo + 24 * 512);
  } else if constexpr (U < 23) {
    constexpr int uu = U - 15;
    const unsigned v = r * 512 + ctr;
    dma4<NT_KV>(c.vc + uu * (32 * 512), dst, v, v + 8 * 512, v + 16 * 512, v + 24 * 512);
  } else if constexpr (U < 31) {
    if (c.w < 7) {
      const int uu = (U - 23 + c.b) & 7;
      const unsigned v = r * 896 + ctr;
      dma4<false>(c.wo + uu * (32 * 896), dst, v, v + 8 * 896, v + 16 * 896, v + 24 * 896);
    }
  }
}

template <int U>
DI void wait_unit(const Ctx& c) {
  if constexpr (U == 21 || U == 22) {
    if (c.w == 7) VMW(0);
  }
  if constexpr (U <= 28) VMW(8);
  else if constexpr (U == 29) VMW(4);
  else VMW(0);
}

template <int U, int TOFF>
DI void tr_unit(const Ctx& c, unsigned abase, const bf16x8 (&bf)[2][2], f32x16 (&acc)[2][2]) {
  wait_unit<U>(c);
  bf16x8 af[2][2];
  const unsigned a0 = abase + (U % 3) * 4096;
#pragma unroll
  for (int s = 0; s < 2; ++s)
#pragma unroll
    for (int cb = 0; cb < 2; ++cb) {
      const unsigned a = (cb ? (a0 ^ 64u) : a0) + s * 2048;
      af[s][cb] = cat(lds_tr(a), lds_tr(a + TOFF));
    }
  LGKM0();
  issue<U + 3>(c);
#pragma unroll
  for (int s = 0; s < 2; ++s)
#pragma unroll
    for (int cb = 0; cb < 2; ++cb)
#pragma unroll
      for (int qb = 0; qb < 2; ++qb) acc[cb][qb] = mfma(af[s][cb], bf[s][qb], acc[cb][qb]);
}

DI void zero_acc(f32x16 (&acc)[2][2]) {
#pragma unroll
  for (int i = 0; i < 2; ++i)
#pragma unroll
    for (int j = 0; j < 2; ++j)
#pragma unroll
      for (int r = 0; r < 16; ++r) acc[i][j][r] = 0.f;
}

DI void exch_send(const Ctx& c, const f32x16 (&acc)[2][2]) {
  const unsigned dst = kRegionA + (unsigned)(c.w ^ 4) * 8192 + c.lane * 16;
#pragma unroll
  for (int db = 0; db < 2; ++db)
#pragma unroll
    for (int r4 = 0; r4 < 4; ++r4) {
      f32x4 v;
#pragma unroll
      for (int j = 0; j < 4; ++j) v[j] = c.kh ? acc[db][0][4 * r4 + j] : acc[db][1][4 * r4 + j];
      sts_f4(dst + (db * 4 + r4) * 1024, v);
    }
}
DI void exch_recv(const Ctx& c, const f32x16 (&acc)[2][2], f32x16 (&t)[2]) {
  const unsigned src = kRegionA + (unsigned)c.w * 8192 + c.lane * 16;
#pragma unroll
  for (int db = 0; db < 2; ++db)
#pragma unroll
    for (int r4 = 0; r4 < 4; ++r4) {
      const f32x4 v = lds_f4(src + (db * 4 + r4) * 1024);
#pragma unroll
      for (int j = 0; j < 4; ++j) t[db][4 * r4 + j] = v[j] + (c.kh ? acc[db][1][4 * r4 + j] : acc[db][0][4 * r4 + j]);
    }
}

template <int U>
DI void q_units(const Ctx& c, unsigned abase, unsigned xrow, unsigned xsw, f32x16 (&acc)[2][2]) {
  if constexpr (U < 7) {
    int uu = U + (c.b == 7 ? 0 : c.b);
    if (uu >= 7) uu -= 7;
    const unsigned ks0 = 14 * c.kh + 2 * uu;
    bf16x8 bf[2][2];
#pragma unroll
    for (int s = 0; s < 2; ++s) {
      const unsigned ks = ks0 + s;
      const unsigned a = kRegionA + (ks >> 2) * 8192 + xrow + (((ks & 3) * 32) ^ xsw);
      bf[s][0] = lds_b128(a);
      bf[s][1] = lds_b128(a + 4096);
    }
    tr_unit<U, 512>(c, abase, bf, acc);
    q_units<U + 1>(c, abase, xrow, xsw, acc);
  }
}

template <int U>
DI void s_units(const Ctx& c, unsigned xrow, unsigned xsw, bf16x8 (&qf)[4][2], f32x16 (&acc)[2][2]) {
  if constexpr (U < 15) {
    constexpr int uu = U - 7, dc = uu >> 1, kb = uu & 1;
    if constexpr (kb == 0) {
#pragma unroll
      for (int s = 0; s < 4; ++s)
#pragma unroll
        for (int qb = 0; qb < 2; ++qb) qf[s][qb] = lds_b128(kRegionA + (dc + 4 * qb) * 8192 + s * 1024 + c.lane * 16);
    }
    wait_unit<U>(c);
    bf16x8 af[4];
#pragma unroll
    for (int s = 0; s < 4; ++s) af[s] = lds_b128(c.ring + (U % 3) * 4096 + xrow + ((s * 32) ^ xsw));
    LGKM0();
    issue<U + 3>(c);
#pragma unroll
    for (int s = 0; s < 4; ++s)
#pragma unroll
      for (int qb = 0; qb < 2; ++qb) acc[kb][qb] = mfma(af[s], qf[s][qb], acc[kb][qb]);
    s_units<U + 1>(c, xrow, xsw, qf, acc);
  }
}

template <int U>
DI void v_units(const Ctx& c, unsigned abase, f32x16 (&acc)[2][2]) {
  if constexpr (U < 23) {
    constexpr int uu = U - 15;
    bf16x8 bf[2][2];
    const unsigned pb = kRegionA + (unsigned)c.kh * 32768 + uu * 4096 + c.lane * 16;
#pragma unroll
    for (int s = 0; s < 2; ++s)
#pragma unroll
      for (int qb = 0; qb < 2; ++qb) bf[s][qb] = lds_b128(pb + s * 2048 + qb * 1024);
    tr_unit<U, 1024>(c, abase, bf, acc);
    v_units<U + 1>(c, abase, acc);
  }
}

template <int U>
DI void o_units(const Ctx& c, unsigned abase, f32x16 (&acc)[2][2]) {
  if constexpr (U < 31) {
    const unsigned uu = (U - 23 + c.b) & 7;
    bf16x8 bf[2][2];
    const unsigned ob = kRegionA + (uu >> 1) * 8192 + (uu & 1) * 2048 + c.lane * 16;
#pragma unroll
    for (int s = 0; s < 2; ++s)
#pragma unroll
      for (int qb = 0; qb < 2; ++qb) bf[s][qb] = lds_b128(ob + s * 1024 + qb * 32768);
    tr_unit<U, 1024>(c, abase, bf, acc);
    o_units<U + 1>(c, abase, acc);
  }
}

__global__ __launch_bounds__(512, 2) void gqa_fused_kernel(
    const char* __restrict__ ax, const char* __restrict__ ak, const char* __restrict__ av, const char* __restrict__ awq,
    const char* __restrict__ awo, const char* __restrict__ bx, const char* __restrict__ bk, const char* __restrict__ bv,
    const char* __restrict__ bwq, const char* __restrict__ bwo, char* __restrict__ part) {
  const int bid = blockIdx.x, xcd = bid & 7, idx = bid >> 3;
  const int br = xcd >> 2, hq = (xcd & 3) * 4 + (idx & 3), b = idx >> 2, hkv = hq >> 1;
  const int lane = threadIdx.x & 63;
  const int w = __builtin_amdgcn_readfirstlane(threadIdx.x >> 6);
  const int ds = w & 3, kh = w >> 2;
  const unsigned l = lane, h = l >> 5;

  STAMP(0);
  Ctx c;
  c.w = w; c.ds = ds; c.kh = kh; c.b = b; c.lane = lane;
  c.ring = kRing + (unsigned)w * 12288;
  c.wq = (br ? bwq : awq) + ((size_t)(224 * kh) * 4096 + hq * 256 + 64 * ds) * 2;
  c.kc = (br ? bk : ak) + ((size_t)((b * kHkv + hkv) * kSk + 64 * w) * kDh) * 2;
  c.vc = (br ? bv : av) + ((size_t)((b * kHkv + hkv) * kSk + 256 * kh) * kDh + 64 * ds) * 2;
  c.wo = (br ? bwo : awo) + ((size_t)(hq * 256) * kD + 64 * w) * 2;

  {
    const char* xs = (br ? bx : ax) + (size_t)(b * kSq + 8 * w) * (kD * 2);
    const unsigned row = 8 * w + (l >> 3);
    const unsigned v = (l >> 3) * 896 + 16 * ((l & 7) ^ ((row >> 1) & 7));
#pragma unroll
    for (int j = 0; j < 7; ++j) dma1(xs + j * 128, kRegionA + j * 8192 + w * 1024, v);
  }
  issue<0>(c);
  issue<1>(c);
  issue<2>(c);

  const unsigned blk = (l >> 4) & 1, q4 = (l & 15) >> 2, p = l & 3, pp = ((p & 1) << 1) | (p >> 1);
  const unsigned trc = c.ring + 32 * blk + 64 * ((q4 >> 1) & 1);
  const unsigned xrow = (l & 31) * 128, xsw = 16 * (h ^ (((l & 31) >> 1) & 7));

  f32x16 acc[2][2];
  f32x16 t[2];
  zero_acc(acc);
  VMW(12);
  BAR();
  STAMP(1);

  q_units<0>(c, trc + 128 * (8 * h + q4) + 8 * pp, xrow, xsw, acc);
  STAMP(2);
  BAR();
  exch_send(c, acc);
  BAR();
  exch_recv(c, acc, t);
  LGKM0();
#pragma unroll
  for (int db = 0; db < 2; ++db)
#pragma unroll
    for (int s = 0; s < 2; ++s) sts_b128(kRegionA + w * 8192 + (2 * db + s) * 1024 + l * 16, pack8(t[db], s));
  zero_acc(acc);
  BAR();

  STAMP(3);
  {
    bf16x8 qf[4][2];
    s_units<7>(c, xrow, xsw, qf, acc);
  }

  STAMP(4);
  float lsum[2];
  {
    float mx[2];
#pragma unroll
    for (int qb = 0; qb < 2; ++qb) {
      float m = acc[0][qb][0];
#pragma unroll
      for (int kb = 0; kb < 2; ++kb)
#pragma unroll
        for (int r = 0; r < 16; ++r) m = fmaxf(m, acc[kb][qb][r]);
      mx[qb] = fmaxf(m, other_half(m));
    }
    sts_f1(kRegionA + w * 8192 + 4096 + l * 4, h ? mx[1] : mx[0]);
    BAR();
    float m = lds_f1(kRegionA + 4096 + l * 4);
#pragma unroll
    for (int i = 1; i < 8; ++i) m = fmaxf(m, lds_f1(kRegionA + i * 8192 + 4096 + l * 4));
    const float mo = other_half(m);
    const float sc = 0.0625f * 1.44269504088896340736f;
    const float m0 = (h ? mo : m) * sc, m1 = (h ? m : mo) * sc;
#pragma unroll
    for (int qb = 0; qb < 2; ++qb) {
      float sum = 0.f;
      const float mm = qb ? m1 : m0;
#pragma unroll
      for (int kb = 0; kb < 2; ++kb)
#pragma unroll
        for (int r = 0; r < 16; ++r) {
          const float e = __builtin_amdgcn_exp2f(fmaf(acc[kb][qb][r], sc, -mm));
          acc[kb][qb][r] = e;
          sum += e;
        }
      lsum[qb] = sum + other_half(sum);
    }
    BAR();
#pragma unroll
    for (int kb = 0; kb < 2; ++kb)
#pragma unroll
      for (int s = 0; s < 2; ++s)
#pragma unroll
        for (int qb = 0; qb < 2; ++qb)
          sts_b128(kRegionA + w * 8192 + ((2 * kb + s) * 2 + qb) * 1024 + l * 16, pack8(acc[kb][qb], s));
    zero_acc(acc);
    BAR();
  }

  STAMP(5);
  v_units<15>(c, trc + 128 * (4 * h + q4) + 8 * p, acc);
  STAMP(6);
  BAR();
  exch_send(c, acc);
  constexpr unsigned kStat = kRing + 7 * 12288;
  sts_f1(kStat + w * 256 + l * 4, h ? lsum[1] : lsum[0]);
  BAR();
  exch_recv(c, acc, t);
  {
    float tot = 0.f;
#pragma unroll
    for (int i = 0; i < 8; ++i) tot += lds_f1(kStat + i * 256 + (32 * kh + (l & 31)) * 4);
    const float inv = 1.0f / tot;
    LGKM0();
#pragma unroll
    for (int db = 0; db < 2; ++db)
#pragma unroll
      for (int s = 0; s < 2; ++s) sts_b128(kRegionA + w * 8192 + (2 * db + s) * 1024 + l * 16, pack8(t[db], s, inv));
  }
  zero_acc(acc);
  BAR();

  STAMP(7);
  if (w < 7) {
    o_units<23>(c, trc + 128 * (4 * h + q4) + 8 * pp, acc);
    STAMP(8);
    char* dst = part + (size_t)((br * 8 + b) * kHq + hq) * kPartBytes + w * 8192 + l * 16;
#pragma unroll
    for (int nb = 0; nb < 2; ++nb)
#pragma unroll
      for (int qb = 0; qb < 2; ++qb)
#pragma unroll
        for (int s = 0; s < 2; ++s) *(bf16x8*)(dst + ((nb * 2 + qb) * 2 + s) * 1024) = pack8(acc[nb][qb], s);
  }
}

__global__ __launch_bounds__(256) void head_reduce_kernel(const char* __restrict__ part, char* __restrict__ out) {
  const int t = blockIdx.x * 256 + threadIdx.x;
  const int g = t / 3584, ck = t % 3584;
  const char* src = part + (size_t)g * kHq * kPartBytes + ck * 16;
  u32x4 v[16];
#pragma unroll
  for (int i = 0; i < 16; ++i) v[i] = *(const u32x4*)(src + (size_t)i * kPartBytes);
  float s[8];
#pragma unroll
  for (int j = 0; j < 8; ++j) s[j] = 0.f;
#pragma unroll
  for (int i = 0; i < 16; ++i)
#pragma unroll
    for (int j = 0; j < 4; ++j) {
      s[2 * j] += __builtin_bit_cast(float, v[i][j] << 16);
      s[2 * j + 1] += __builtin_bit_cast(float, v[i][j] & 0xffff0000u);
    }
  bf16x8 r;
#pragma unroll
  for (int j = 0; j < 4; ++j) {
    f32x2 pr = {s[2 * j], s[2 * j + 1]};
    bf16x2 cv = __builtin_convertvector(pr, bf16x2);
    r[2 * j] = cv[0];
    r[2 * j + 1] = cv[1];
  }
  const int lane = ck & 63, fr = (ck >> 6) & 7, w = ck >> 9;
  const int nb = fr >> 2, qb = (fr >> 1) & 1, sh = fr & 1;
  const int q = 32 * qb + (lane & 31), n = 64 * w + 32 * nb + 16 * sh + 8 * (lane >> 5);
  const int br = g >> 3, b = g & 7;
  *(bf16x8*)(out + ((size_t)(b * kSq + q) * (2 * kD) + br * kD + n) * 2) = r;
}

extern "C" void kernel_launch(void* const* d_in, const int* in_sizes, int n_in, void* d_out, int out_size, void* d_ws,
                              size_t ws_size, hipStream_t stream) {
  (void)in_sizes; (void)n_in; (void)out_size; (void)ws_size;
  (void)hipFuncSetAttribute((const void*)gqa_fused_kernel, hipFuncAttributeMaxDynamicSharedMemorySize, kLdsBytes);
  gqa_fused_kernel<<<256, 512, kLdsBytes, stream>>>(
      (const char*)d_in[0], (const char*)d_in[1], (const char*)d_in[2], (const char*)d_in[3], (const char*)d_in[4],
      (const char*)d_in[5], (const char*)d_in[6], (const char*)d_in[7], (const char*)d_in[8], (const char*)d_in[9],
      (char*)d_ws);
  head_reduce_kernel<<<224, 256, 0, stream>>>((const char*)d_ws, (char*)d_out);
}
```

```cpp
#include <hip/hip_runtime.h>
#include <hip/hip_bf16.h>

typedef __attribute__((ext_vector_type(8))) __bf16 bf16x8;
typedef __attribute__((ext_vector_type(4))) __bf16 bf16x4;
typedef __attribute__((ext_vector_type(2))) __bf16 bf16x2;
typedef __attribute__((ext_vector_type(16))) float f32x16;
typedef __attribute__((ext_vector_type(4))) float f32x4;
typedef __attribute__((ext_vector_type(2))) float f32x2;
typedef __attribute__((ext_vector_type(4))) unsigned u32x4;
#define LDS_AS __attribute__((address_space(3)))
#define DI __device__ __forceinline__

#define NT_KV 1

constexpr int kD = 448, kHq = 16, kHkv = 8, kSk = 512, kDh = 256, kSq = 64;
constexpr unsigned kRegionA = 0;
constexpr unsigned kRing = 65536;
constexpr unsigned kLdsBytes = 163840;
constexpr int kPartBytes = 7 * 8192;

#define BAR() asm volatile("s_waitcnt lgkmcnt(0)\n\ts_barrier" ::: "memory")
#define LGKM0() asm volatile("s_waitcnt lgkmcnt(0)" ::: "memory")
#define VMW(n) asm volatile("s_waitcnt vmcnt(" #n ")" ::: "memory")
#define STAMP(i)

template <bool NT>
DI void dma4(const char* sbase, unsigned ldsdst, unsigned v0, unsigned v1, unsigned v2, unsigned v3) {
  unsigned keep;
  if (NT) {
    asm volatile(
        "s_mov_b32 %0, m0\n\ts_mov_b32 m0, %6\n\ts_nop 0\n\t"
        "global_load_lds_dwordx4 %1, %5 nt\n\ts_add_u32 m0, m0, 0x400\n\ts_nop 0\n\t"
        "global_load_lds_dwordx4 %2, %5 nt\n\ts_add_u32 m0, m0, 0x400\n\ts_nop 0\n\t"
        "global_load_lds_dwordx4 %3, %5 nt\n\ts_add_u32 m0, m0, 0x400\n\ts_nop 0\n\t"
        "global_load_lds_dwordx4 %4, %5 nt\n\ts_mov_b32 m0, %0"
        : "=&s"(keep)
        : "v"(v0), "v"(v1), "v"(v2), "v"(v3), "s"(sbase), "s"(ldsdst)
        : "memory", "scc");
  } else {
    asm volatile(
        "s_mov_b32 %0, m0\n\ts_mov_b32 m0, %6\n\ts_nop 0\n\t"
        "global_load_lds_dwordx4 %1, %5\n\ts_add_u32 m0, m0, 0x400\n\ts_nop 0\n\t"
        "global_load_lds_dwordx4 %2, %5\n\ts_add_u32 m0, m0, 0x400\n\ts_nop 0\n\t"
        "global_load_lds_dwordx4 %3, %5\n\ts_add_u32 m0, m0, 0x400\n\ts_nop 0\n\t"
        "global_load_lds_dwordx4 %4, %5\n\ts_mov_b32 m0, %0"
        : "=&s"(keep)
        : "v"(v0), "v"(v1), "v"(v2), "v"(v3), "s"(sbase), "s"(ldsdst)
        : "memory", "scc");
  }
}

DI void dma1(const char* sbase, unsigned ldsdst, unsigned v0) {
  unsigned keep;
  asm volatile("s_mov_b32 %0, m0\n\ts_mov_b32 m0, %3\n\ts_nop 0\n\tglobal_load_lds_dwordx4 %1, %2\n\ts_mov_b32 m0, %0"
               : "=&s"(keep)
               : "v"(v0), "s"(sbase), "s"(ldsdst)
               : "memory");
}

DI bf16x8 lds_b128(unsigned a) { return *(const LDS_AS bf16x8*)(size_t)a; }
DI f32x4 lds_f4(unsigned a) { return *(const LDS_AS f32x4*)(size_t)a; }
DI float lds_f1(unsigned a) { return *(const LDS_AS float*)(size_t)a; }
DI void sts_b128(unsigned a, bf16x8 v) { *(LDS_AS bf16x8*)(size_t)a = v; }
DI void sts_f4(unsigned a, f32x4 v) { *(LDS_AS f32x4*)(size_t)a = v; }
DI void sts_f1(unsigned a, float v) { *(LDS_AS float*)(size_t)a = v; }
DI bf16x4 lds_tr(unsigned a) { return __builtin_amdgcn_ds_read_tr16_b64_v4bf16((LDS_AS bf16x4*)(size_t)a); }
DI bf16x8 cat(bf16x4 a, bf16x4 b) { return __builtin_shufflevector(a, b, 0, 1, 2, 3, 4, 5, 6, 7); }
DI f32x16 mfma(bf16x8 a, bf16x8 b, f32x16 c) { return __builtin_amdgcn_mfma_f32_32x32x16_bf16(a, b, c, 0, 0, 0); }

DI bf16x8 pack8(const f32x16& t, int s, float scale) {
  bf16x8 r;
#pragma unroll
  for (int j = 0; j < 4; ++j) {
    f32x2 p = {t[8 * s + 2 * j] * scale, t[8 * s + 2 * j + 1] * scale};
    bf16x2 c = __builtin_convertvector(p, bf16x2);
    r[2 * j] = c[0];
    r[2 * j + 1] = c[1];
  }
  return r;
}
DI bf16x8 pack8(const f32x16& t, int s) {
  bf16x8 r;
#pragma unroll
  for (int j = 0; j < 4; ++j) {
    f32x2 p = {t[8 * s + 2 * j], t[8 * s + 2 * j + 1]};
    bf16x2 c = __builtin_convertvector(p, bf16x2);
    r[2 * j] = c[0];
    r[2 * j + 1] = c[1];
  }
  return r;
}
DI float other_half(float v) { return __shfl_xor(v, 32, 64); }

template <int N>
DI void vmwait() { asm volatile("s_waitcnt vmcnt(%0)" ::"n"(N) : "memory"); }

struct Unit { u32x4 r[4]; };
struct Off4 { unsigned v[4]; };

template <bool NT>
DI void get_unit(Unit& u, const char* sbase, const Off4& o) {
  if (NT) {
    asm volatile(
        "global_load_dwordx4 %0, %4, %8 nt\n\tglobal_load_dwordx4 %1, %5, %8 nt\n\t"
        "global_load_dwordx4 %2, %6, %8 nt\n\tglobal_load_dwordx4 %3, %7, %8 nt"
        : "=&v"(u.r[0]), "=&v"(u.r[1]), "=&v"(u.r[2]), "=&v"(u.r[3])
        : "v"(o.v[0]), "v"(o.v[1]), "v"(o.v[2]), "v"(o.v[3]), "s"(sbase)
        : "memory");
  } else {
    asm volatile(
        "global_load_dwordx4 %0, %4, %8\n\tglobal_load_dwordx4 %1, %5, %8\n\t"
        "global_load_dwordx4 %2, %6, %8\n\tglobal_load_dwordx4 %3, %7, %8"
        : "=&v"(u.r[0]), "=&v"(u.r[1]), "=&v"(u.r[2]), "=&v"(u.r[3])
        : "v"(o.v[0]), "v"(o.v[1]), "v"(o.v[2]), "v"(o.v[3]), "s"(sbase)
        : "memory");
  }
}
template <int N>
DI void put_unit(const Unit& u, unsigned addr) {
  asm volatile(
      "s_waitcnt vmcnt(%5)\n\tds_write_b128 %0, %1\n\tds_write_b128 %0, %2 offset:1024\n\t"
      "ds_write_b128 %0, %3 offset:2048\n\tds_write_b128 %0, %4 offset:3072"
      :
      : "v"(addr), "v"(u.r[0]), "v"(u.r[1]), "v"(u.r[2]), "v"(u.r[3]), "n"(N)
      : "memory");
}
template <bool NT>
DI void dma_unit(const char* sbase, unsigned dst, const Off4& o) { dma4<NT>(sbase, dst, o.v[0], o.v[1], o.v[2], o.v[3]); }

DI void zero16(f32x16& a) {
#pragma unroll
  for (int r = 0; r < 16; ++r) a[r] = 0.f;
}

template <int TOFF>
DI void stage_mma(unsigned abase, const bf16x8 (&bf)[4][2], f32x16 (&acc)[2]) {
  bf16x8 af[4];
#pragma unroll
  for (int s = 0; s < 4; ++s) af[s] = cat(lds_tr(abase + s * 8192), lds_tr(abase + s * 8192 + TOFF));
#pragma unroll
  for (int s = 0; s < 4; ++s)
#pragma unroll
    for (int qb = 0; qb < 2; ++qb) acc[qb] = mfma(af[s], bf[s][qb], acc[qb]);
}

__global__ __launch_bounds__(512, 2) void gqa_fused_kernel(
    const char* __restrict__ ax, const char* __restrict__ ak, const char* __restrict__ av, const char* __restrict__ awq,
    const char* __restrict__ awo, const char* __restrict__ bx, const char* __restrict__ bk, const char* __restrict__ bv,
    const char* __restrict__ bwq, const char* __restrict__ bwo, char* __restrict__ part) {
  const int bid = blockIdx.x, xcd = bid & 7, idx = bid >> 3;
  const int br = xcd >> 2, hq = (xcd & 3) * 4 + (idx & 3), b = idx >> 2, hkv = hq >> 1;
  const unsigned l = threadIdx.x & 63, h = l >> 5;
  const int w = __builtin_amdgcn_readfirstlane(threadIdx.x >> 6);
  STAMP(0);
  const char* wq = (br ? bwq : awq) + ((size_t)(8 * w) * 4096 + hq * 256) * 2;
  const char* kc = (br ? bk : ak) + ((size_t)((b * kHkv + hkv) * kSk + 64 * w) * kDh) * 2;
  const char* vc = (br ? bv : av) + ((size_t)((b * kHkv + hkv) * kSk + 8 * w) * kDh) * 2;
  const char* wo = (br ? bwo : awo) + ((size_t)(hq * 256) * kD + 64 * w) * 2;
  const unsigned mine = kRing + (unsigned)w * 4096;
  const unsigned r8 = l >> 3, ch = l & 7, ctr = 16 * (ch ^ (4 * ((l >> 4) & 1)));
  const int rq = b == 7 ? 0 : b;

  {
    const char* xs = (br ? bx : ax) + (size_t)(b * kSq + 8 * w) * (kD * 2);
    const unsigned row = 8 * w + r8;
    const unsigned v = r8 * 896 + 16 * (ch ^ ((row >> 1) & 7));
#pragma unroll
    for (int j = 0; j < 7; ++j) dma1(xs + j * 128, kRegionA + j * 8192 + w * 1024, v);
  }
  Off4 oq, ok, ov, oo;
#pragma unroll
  for (int i = 0; i < 4; ++i) {
    oq.v[i] = r8 * 8192 + ctr + i * 128;
    ok.v[i] = (r8 + 8 * i) * 512 + 16 * (ch ^ ((4 * i + (l >> 4)) & 7));
  }
  auto wq_stage = [&](int u) { int uu = u + rq; return uu >= 7 ? uu - 7 : uu; };
  dma_unit<false>(wq + (size_t)wq_stage(0) * (64 * 8192), mine, oq);
  asm volatile("s_barrier" ::: "memory");
  dma_unit<false>(wq + (size_t)wq_stage(1) * (64 * 8192), mine + 32768, oq);

  const unsigned blk = (l >> 4) & 1, q4 = (l & 15) >> 2, p = l & 3, pp = ((p & 1) << 1) | (p >> 1);
  const unsigned trl = 32 * blk + 128 * q4;
  const unsigned trs = kRing + (unsigned)(w >> 1) * 1024 + trl + 64 * ((w & 1) ^ ((q4 >> 1) & 1));
  const unsigned xrow = (l & 31) * 128, xsw = 16 * (h ^ (((l & 31) >> 1) & 7));
  const unsigned blockA = kRegionA + (unsigned)w * 8192 + l * 16;

  Unit R[6];
  f32x16 acc[2][2];
  zero16(acc[0][0]); zero16(acc[0][1]);

#pragma unroll
  for (int u = 0; u < 7; ++u) {
    if (u <= 4) vmwait<4>(); else vmwait<28>();
    BAR();
    if (u == 0) STAMP(1);
    if (u <= 4) dma_unit<false>(wq + (size_t)wq_stage(u + 2) * (64 * 8192), mine + ((u + 2) % 3) * 32768, oq);
    if (u == 4) {
#pragma unroll
      for (int j = 0; j < 6; ++j) get_unit<NT_KV>(R[j], kc + (j & 1) * (32 * 512) + (j >> 1) * 128, ok);
    }
    if (u >= 5) dma_unit<NT_KV>(kc + (u - 5) * (32 * 512) + 3 * 128, mine + (u - 4) * 32768, ok);
    bf16x8 bf[4][2];
    const unsigned xa = kRegionA + wq_stage(u) * 8192 + xrow;
#pragma unroll
    for (int s = 0; s < 4; ++s) {
      bf[s][0] = lds_b128(xa + ((s * 32) ^ xsw));
      bf[s][1] = lds_b128(xa + ((s * 32) ^ xsw) + 4096);
    }
    stage_mma<512>(trs + (u % 3) * 32768 + h * 4096 + 8 * pp, bf, acc[0]);
  }
  STAMP(2);
#pragma unroll
  for (int i = 0; i < 4; ++i) ov.v[i] = r8 * 512 + ctr + i * 128;
  BAR();
  dma_unit<NT_KV>(vc, mine, ov);
#pragma unroll
  for (int s = 0; s < 2; ++s)
#pragma unroll
    for (int qb = 0; qb < 2; ++qb) sts_b128(blockA + (s * 2 + qb) * 1024, pack8(acc[0][qb], s));
  zero16(acc[0][0]); zero16(acc[0][1]); zero16(acc[1][0]); zero16(acc[1][1]);
  BAR();
  STAMP(3);

  bf16x8 qf[4][2];
#pragma unroll
  for (int j = 0; j < 8; ++j) {
    const int dc = j >> 1, kb = j & 1;
    bf16x8 af[4];
    if (kb == 0) {
#pragma unroll
      for (int s = 0; s < 4; ++s)
#pragma unroll
        for (int qb = 0; qb < 2; ++qb)
          qf[s][qb] = lds_b128(kRegionA + (2 * dc + (s >> 1)) * 8192 + ((s & 1) * 2 + qb) * 1024 + l * 16);
    }
    unsigned src;
    if (j < 6) {
      put_unit<32>(R[j], blockA + 4096);
      get_unit<NT_KV>(R[j], vc + (j + 1) * (64 * 512), ov);
      src = kRegionA + (unsigned)w * 8192 + 4096;
    } else {
      vmwait<32>();
      src = mine + (j - 5) * 32768;
    }
#pragma unroll
    for (int s = 0; s < 4; ++s) af[s] = lds_b128(src + xrow + ((s * 32) ^ xsw));
    if (j == 6) {
      LGKM0();
      dma_unit<NT_KV>(vc + 7 * (64 * 512), mine + 32768, ov);
    }
#pragma unroll
    for (int s = 0; s < 4; ++s)
#pragma unroll
      for (int qb = 0; qb < 2; ++qb) {
        acc[kb][qb] = mfma(af[s], qf[s][qb], acc[kb][qb]);
        if (kb == 1 && s == 2 && qb == 1) __builtin_amdgcn_sched_barrier(0);
      }
  }
  STAMP(4);

#pragma unroll
  for (int i = 0; i < 4; ++i) oo.v[i] = (r8 + 8 * i) * 896 + ctr;
  const bool ow = w < 7;
  const char* wos = ow ? wo : wo - 128;
  float lsum[2];
  {
    float mx[2];
#pragma unroll
    for (int qb = 0; qb < 2; ++qb) {
      float m = acc[0][qb][0];
#pragma unroll
      for (int kb = 0; kb < 2; ++kb)
#pragma unroll
        for (int r = 0; r < 16; ++r) m = fmaxf(m, acc[kb][qb][r]);
      mx[qb] = fmaxf(m, other_half(m));
    }
    sts_f1(kRegionA + w * 8192 + 4096 + l * 4, h ? mx[1] : mx[0]);
    BAR();
    float m = lds_f1(kRegionA + 4096 + l * 4);
#pragma unroll
    for (int i = 1; i < 8; ++i) m = fmaxf(m, lds_f1(kRegionA + i * 8192 + 4096 + l * 4));
    const float mo = other_half(m);
    const float sc = 0.0625f * 1.44269504088896340736f;
    const float m0 = (h ? mo : m) * sc, m1 = (h ? m : mo) * sc;
    BAR();
#pragma unroll
    for (int kb = 0; kb < 2; ++kb)
#pragma unroll
      for (int qb = 0; qb < 2; ++qb) {
        float sum = kb ? lsum[qb] : 0.f;
        const float mm = qb ? m1 : m0;
#pragma unroll
        for (int r = 0; r < 16; ++r) {
          const float e = __builtin_amdgcn_exp2f(fmaf(acc[kb][qb][r], sc, -mm));
          acc[kb][qb][r] = e;
          sum += e;
        }
        lsum[qb] = sum;
#pragma unroll
        for (int s = 0; s < 2; ++s) sts_b128(blockA + ((2 * kb + s) * 2 + qb) * 1024, pack8(acc[kb][qb], s));
      }
    lsum[0] += other_half(lsum[0]);
    lsum[1] += other_half(lsum[1]);
    zero16(acc[0][0]); zero16(acc[0][1]);
  }
  STAMP(5);

  constexpr unsigned kStat = kRing + 2 * 32768 + 7 * 4096;
#pragma unroll
  for (int j = 0; j < 8; ++j) {
    if (j == 0) vmwait<28>();
    if (j == 7) vmwait<28>();
    BAR();
    if (j < 6) {
      put_unit<24>(R[j], mine + ((j & 1) ? 0 : 65536) + l * 16);
      get_unit<false>(R[j], wos + ((j + b) & 7) * (32 * 896), oo);
    }
    if (j == 6) {
      if (ow) dma_unit<false>(wo + ((6 + b) & 7) * (32 * 896), mine + 65536, oo);
      else vmwait<24>();
      sts_f1(kStat + w * 256 + l * 4, h ? lsum[1] : lsum[0]);
    }
    bf16x8 bf[4][2];
#pragma unroll
    for (int s = 0; s < 4; ++s)
#pragma unroll
      for (int qb = 0; qb < 2; ++qb) bf[s][qb] = lds_b128(kRegionA + j * 8192 + (s * 2 + qb) * 1024 + l * 16);
    stage_mma<4096>(trs + (j == 7 ? 32768 : (j & 1) * 65536) + h * 512 + 8 * p, bf, acc[0]);
  }
  STAMP(6);
#pragma unroll
  for (int qb = 0; qb < 2; ++qb) {
    float tot = 0.f;
#pragma unroll
    for (int i = 0; i < 8; ++i) tot += lds_f1(kStat + i * 256 + (32 * qb + (l & 31)) * 4);
    const float inv = 1.0f / tot;
#pragma unroll
    for (int s = 0; s < 2; ++s) sts_b128(mine + (s * 2 + qb) * 1024 + l * 16, pack8(acc[0][qb], s, inv));
  }
  zero16(acc[0][0]); zero16(acc[0][1]); zero16(acc[1][0]); zero16(acc[1][1]);
  BAR();
  STAMP(7);

  if (ow) {
    dma_unit<false>(wo + ((7 + b) & 7) * (32 * 896), mine + 32768, oo);
    put_unit<28>(R[0], blockA);
    const unsigned tro = trl + 64 * ((q4 >> 1) & 1) + h * 512 + 8 * pp;
#pragma unroll
    for (int j = 0; j < 8; ++j) {
      if (j == 0) put_unit<24>(R[1], blockA + 4096);
      if (j == 6) vmwait<4>();
      if (j == 7) vmwait<0>();
      const unsigned uu = (j + b) & 7;
      const unsigned src = (j < 6 ? kRegionA + (unsigned)w * 8192 + (j & 1) * 4096 : mine + (j == 6 ? 65536 : 32768)) + tro;
      bf16x8 bf[2][2], af[2][2];
#pragma unroll
      for (int s = 0; s < 2; ++s) {
#pragma unroll
        for (int qb = 0; qb < 2; ++qb) bf[s][qb] = lds_b128(kRing + uu * 4096 + (s * 2 + qb) * 1024 + l * 16);
#pragma unroll
        for (int cb = 0; cb < 2; ++cb) {
          const unsigned a = (cb ? (src ^ 64u) : src) + s * 2048;
          af[s][cb] = cat(lds_tr(a), lds_tr(a + 1024));
        }
      }
      if (j == 0) put_unit<20>(R[2], blockA);
      if (j == 1) put_unit<16>(R[3], blockA + 4096);
      if (j == 2) put_unit<12>(R[4], blockA);
      if (j == 3) put_unit<8>(R[5], blockA + 4096);
#pragma unroll
      for (int s = 0; s < 2; ++s)
#pragma unroll
        for (int cb = 0; cb < 2; ++cb)
#pragma unroll
          for (int qb = 0; qb < 2; ++qb) acc[cb][qb] = mfma(af[s][cb], bf[s][qb], acc[cb][qb]);
    }
    STAMP(8);
    char* dst = part + (size_t)((br * 8 + b) * kHq + hq) * kPartBytes + w * 8192 + l * 16;
#pragma unroll
    for (int nb = 0; nb < 2; ++nb)
#pragma unroll
      for (int qb = 0; qb < 2; ++qb)
#pragma unroll
        for (int s = 0; s < 2; ++s) *(bf16x8*)(dst + ((nb * 2 + qb) * 2 + s) * 1024) = pack8(acc[nb][qb], s);
  } else {
    vmwait<0>();
  }
}

__global__ __launch_bounds__(256) void head_reduce_kernel(const char* __restrict__ part, char* __restrict__ out) {
  const int t = blockIdx.x * 256 + threadIdx.x;
  const int g = t / 3584, ck = t % 3584;
  const char* src = part + (size_t)g * kHq * kPartBytes + ck * 16;
  u32x4 v[16];
#pragma unroll
  for (int i = 0; i < 16; ++i) v[i] = *(const u32x4*)(src + (size_t)i * kPartBytes);
  float s[8];
#pragma unroll
  for (int j = 0; j < 8; ++j) s[j] = 0.f;
#pragma unroll
  for (int i = 0; i < 16; ++i)
#pragma unroll
    for (int j = 0; j < 4; ++j) {
      s[2 * j] += __builtin_bit_cast(float, v[i][j] << 16);
      s[2 * j + 1] += __builtin_bit_cast(float, v[i][j] & 0xffff0000u);
    }
  bf16x8 r;
#pragma unroll
  for (int j = 0; j < 4; ++j) {
    f32x2 pr = {s[2 * j], s[2 * j + 1]};
    bf16x2 cv = __builtin_convertvector(pr, bf16x2);
    r[2 * j] = cv[0];
    r[2 * j + 1] = cv[1];
  }
  const int lane = ck & 63, fr = (ck >> 6) & 7, w = ck >> 9;
  const int nb = fr >> 2, qb = (fr >> 1) & 1, sh = fr & 1;
  const int q = 32 * qb + (lane & 31), n = 64 * w + 32 * nb + 16 * sh + 8 * (lane >> 5);
  const int br = g >> 3, b = g & 7;
  *(bf16x8*)(out + ((size_t)(b * kSq + q) * (2 * kD) + br * kD + n) * 2) = r;
}

extern "C" void kernel_launch(void* const* d_in, const int* in_sizes, int n_in, void* d_out, int out_size, void* d_ws,
                              size_t ws_size, hipStream_t stream) {
  (void)in_sizes; (void)n_in; (void)out_size; (void)ws_size;
  (void)hipFuncSetAttribute((const void*)gqa_fused_kernel, hipFuncAttributeMaxDynamicSharedMemorySize, kLdsBytes);
  gqa_fused_kernel<<<256, 512, kLdsBytes, stream>>>(
      (const char*)d_in[0], (const char*)d_in[1], (const char*)d_in[2], (const char*)d_in[3], (const char*)d_in[4],
      (const char*)d_in[5], (const char*)d_in[6], (const char*)d_in[7], (const char*)d_in[8], (const char*)d_in[9],
      (char*)d_ws);
  head_reduce_kernel<<<224, 256, 0, stream>>>((const char*)d_ws, (char*)d_out);
}
```

```cpp
#include <hip/hip_runtime.h>
#include <hip/hip_bf16.h>

typedef __attribute__((ext_vector_type(8))) __bf16 bf16x8;
typedef __attribute__((ext_vector_type(4))) __bf16 bf16x4;
typedef __attribute__((ext_vector_type(2))) __bf16 bf16x2;
typedef __attribute__((ext_vector_type(16))) float f32x16;
typedef __attribute__((ext_vector_type(4))) float f32x4;
typedef __attribute__((ext_vector_type(2))) float f32x2;
typedef __attribute__((ext_vector_type(4))) unsigned u32x4;
#define LDS_AS __attribute__((address_space(3)))
#define DI __device__ __forceinline__

#define NT_KV 1

constexpr int kD = 448, kHq = 16, kHkv = 8, kSk = 512, kDh = 256, kSq = 64;
constexpr unsigned kRegionA = 0;
constexpr unsigned kRing = 65536;
constexpr unsigned kLdsBytes = 163840;
constexpr int kPartBytes = 7 * 8192;

#define BAR() asm volatile("s_waitcnt lgkmcnt(0)\n\ts_barrier" ::: "memory")
#define LGKM0() asm volatile("s_waitcnt lgkmcnt(0)" ::: "memory")
#define VMW(n) asm volatile("s_waitcnt vmcnt(" #n ")" ::: "memory")
#define STAMP(i)

template <bool NT>
DI void dma4(const char* sbase, unsigned ldsdst, unsigned v0, unsigned v1, unsigned v2, unsigned v3) {
  unsigned keep;
  if (NT) {
    asm volatile(
        "s_mov_b32 %0, m0\n\ts_mov_b32 m0, %6\n\ts_nop 0\n\t"
        "global_load_lds_dwordx4 %1, %5 nt\n\ts_add_u32 m0, m0, 0x400\n\ts_nop 0\n\t"
        "global_load_lds_dwordx4 %2, %5 nt\n\ts_add_u32 m0, m0, 0x400\n\ts_nop 0\n\t"
        "global_load_lds_dwordx4 %3, %5 nt\n\ts_add_u32 m0, m0, 0x400\n\ts_nop 0\n\t"
        "global_load_lds_dwordx4 %4, %5 nt\n\ts_mov_b32 m0, %0"
        : "=&s"(keep)
        : "v"(v0), "v"(v1), "v"(v2), "v"(v3), "s"(sbase), "s"(ldsdst)
        : "memory", "scc");
  } else {
    asm volatile(
        "s_mov_b32 %0, m0\n\ts_mov_b32 m0, %6\n\ts_nop 0\n\t"
        "global_load_lds_dwordx4 %1, %5\n\ts_add_u32 m0, m0, 0x400\n\ts_nop 0\n\t"
        "global_load_lds_dwordx4 %2, %5\n\ts_add_u32 m0, m0, 0x400\n\ts_nop 0\n\t"
        "global_load_lds_dwordx4 %3, %5\n\ts_add_u32 m0, m0, 0x400\n\ts_nop 0\n\t"
        "global_load_lds_dwordx4 %4, %5\n\ts_mov_b32 m0, %0"
        : "=&s"(keep)
        : "v"(v0), "v"(v1), "v"(v2), "v"(v3), "s"(sbase), "s"(ldsdst)
        : "memory", "scc");
  }
}

DI void dma1(const char* sbase, unsigned ldsdst, unsigned v0) {
  unsigned keep;
  asm volatile("s_mov_b32 %0, m0\n\ts_mov_b32 m0, %3\n\ts_nop 0\n\tglobal_load_lds_dwordx4 %1, %2\n\ts_mov_b32 m0, %0"
               : "=&s"(keep)
               : "v"(v0), "s"(sbase), "s"(ldsdst)
               : "memory");
}

DI bf16x8 lds_b128(unsigned a) { return *(const LDS_AS bf16x8*)(size_t)a; }
DI f32x4 lds_f4(unsigned a) { return *(const LDS_AS f32x4*)(size_t)a; }
DI float lds_f1(unsigned a) { return *(const LDS_AS float*)(size_t)a; }
DI void sts_b128(unsigned a, bf16x8 v) { *(LDS_AS bf16x8*)(size_t)a = v; }
DI void sts_f4(unsigned a, f32x4 v) { *(LDS_AS f32x4*)(size_t)a = v; }
DI void sts_f1(unsigned a, float v) { *(LDS_AS float*)(size_t)a = v; }
DI bf16x4 lds_tr(unsigned a) { return __builtin_amdgcn_ds_read_tr16_b64_v4bf16((LDS_AS bf16x4*)(size_t)a); }
DI bf16x8 cat(bf16x4 a, bf16x4 b) { return __builtin_shufflevector(a, b, 0, 1, 2, 3, 4, 5, 6, 7); }
DI f32x16 mfma(bf16x8 a, bf16x8 b, f32x16 c) { return __builtin_amdgcn_mfma_f32_32x32x16_bf16(a, b, c, 0, 0, 0); }

DI bf16x8 pack8(const f32x16& t, int s, float scale) {
  bf16x8 r;
#pragma unroll
  for (int j = 0; j < 4; ++j) {
    f32x2 p = {t[8 * s + 2 * j] * scale, t[8 * s + 2 * j + 1] * scale};
    bf16x2 c = __builtin_convertvector(p, bf16x2);
    r[2 * j] = c[0];
    r[2 * j + 1] = c[1];
  }
  return r;
}
DI bf16x8 pack8(const f32x16& t, int s) {
  bf16x8 r;
#pragma unroll
  for (int j = 0; j < 4; ++j) {
    f32x2 p = {t[8 * s + 2 * j], t[8 * s + 2 * j + 1]};
    bf16x2 c = __builtin_convertvector(p, bf16x2);
    r[2 * j] = c[0];
    r[2 * j + 1] = c[1];
  }
  return r;
}
DI float other_half(float v) { return __shfl_xor(v, 32, 64); }

template <int N>
DI void vmwait() { asm volatile("s_waitcnt vmcnt(%0)" ::"n"(N) : "memory"); }

struct Unit { u32x4 r[4]; };
struct Off4 { unsigned v[4]; };

template <bool NT>
DI void get_unit(Unit& u, const char* sbase, const Off4& o) {
  if (NT) {
    asm volatile(
        "global_load_dwordx4 %0, %4, %8 nt\n\tglobal_load_dwordx4 %1, %5, %8 nt\n\t"
        "global_load_dwordx4 %2, %6, %8 nt\n\tglobal_load_dwordx4 %3, %7, %8 nt"
        : "=&v"(u.r[0]), "=&v"(u.r[1]), "=&v"(u.r[2]), "=&v"(u.r[3])
        : "v"(o.v[0]), "v"(o.v[1]), "v"(o.v[2]), "v"(o.v[3]), "s"(sbase)
        : "memory");
  } else {
    asm volatile(
        "global_load_dwordx4 %0, %4, %8\n\tglobal_load_dwordx4 %1, %5, %8\n\t"
        "global_load_dwordx4 %2, %6, %8\n\tglobal_load_dwordx4 %3, %7, %8"
        : "=&v"(u.r[0]), "=&v"(u.r[1]), "=&v"(u.r[2]), "=&v"(u.r[3])
        : "v"(o.v[0]), "v"(o.v[1]), "v"(o.v[2]), "v"(o.v[3]), "s"(sbase)
        : "memory");
  }
}
template <int N>
DI void put_unit(const Unit& u, unsigned addr) {
  asm volatile(
      "s_waitcnt vmcnt(%5)\n\tds_write_b128 %0, %1\n\tds_write_b128 %0, %2 offset:1024\n\t"
      "ds_write_b128 %0, %3 offset:2048\n\tds_write_b128 %0, %4 offset:3072"
      :
      : "v"(addr), "v"(u.r[0]), "v"(u.r[1]), "v"(u.r[2]), "v"(u.r[3]), "n"(N)
      : "memory");
}
template <bool NT>
DI void dma_unit(const char* sbase, unsigned dst, const Off4& o) { dma4<NT>(sbase, dst, o.v[0], o.v[1], o.v[2], o.v[3]); }

DI void zero16(f32x16& a) {
#pragma unroll
  for (int r = 0; r < 16; ++r) a[r] = 0.f;
}

template <int N>
DI void lgkmwait() { asm volatile("s_waitcnt lgkmcnt(%0)" ::"n"(N) : "memory"); }

struct Frag { bf16x8 a[4], b[4][2]; };

template <int TOFF>
DI void read_a(Frag& f, unsigned abase) {
#pragma unroll
  for (int s = 0; s < 4; ++s) f.a[s] = cat(lds_tr(abase + s * 8192), lds_tr(abase + s * 8192 + TOFF));
}
DI void stage_mma(const Frag& f, f32x16 (&acc)[2]) {
#pragma unroll
  for (int s = 0; s < 4; ++s)
#pragma unroll
    for (int qb = 0; qb < 2; ++qb) acc[qb] = mfma(f.a[s], f.b[s][qb], acc[qb]);
}

__global__ __launch_bounds__(512, 2) void gqa_fused_kernel(
    const char* __restrict__ ax, const char* __restrict__ ak, const char* __restrict__ av, const char* __restrict__ awq,
    const char* __restrict__ awo, const char* __restrict__ bx, const char* __restrict__ bk, const char* __restrict__ bv,
    const char* __restrict__ bwq, const char* __restrict__ bwo, char* __restrict__ part) {
  const int bid = blockIdx.x, xcd = bid & 7, idx = bid >> 3;
  const int br = xcd >> 2, hq = (xcd & 3) * 4 + (idx & 3), b = idx >> 2, hkv = hq >> 1;
  const unsigned l = threadIdx.x & 63, h = l >> 5;
  const int w = __builtin_amdgcn_readfirstlane(threadIdx.x >> 6);
  STAMP(0);
  const char* wq = (br ? bwq : awq) + ((size_t)(8 * w) * 4096 + hq * 256) * 2;
  const char* kc = (br ? bk : ak) + ((size_t)((b * kHkv + hkv) * kSk + 64 * w) * kDh) * 2;
  const char* vc = (br ? bv : av) + ((size_t)((b * kHkv + hkv) * kSk + 8 * w) * kDh) * 2;
  const char* wo = (br ? bwo : awo) + ((size_t)(hq * 256) * kD + 64 * w) * 2;
  const unsigned mine = kRing + (unsigned)w * 4096;
  const unsigned r8 = l >> 3, ch = l & 7, ctr = 16 * (ch ^ (4 * ((l >> 4) & 1)));
  const int rq = b == 7 ? 0 : b;
  const bool ow = w < 7;

  {
    const char* xs = (br ? bx : ax) + (size_t)(b * kSq + 8 * w) * (kD * 2);
    const unsigned row = 8 * w + r8;
    const unsigned v = r8 * 896 + 16 * (ch ^ ((row >> 1) & 7));
#pragma unroll
    for (int j = 0; j < 7; ++j) dma1(xs + j * 128, kRegionA + j * 8192 + w * 1024, v);
  }
  Off4 oq, ok, ov, oo;
#pragma unroll
  for (int i = 0; i < 4; ++i) oq.v[i] = r8 * 8192 + ctr + i * 128;
  auto wq_stage = [&](int u) { int uu = u + rq; return uu >= 7 ? uu - 7 : uu; };
  dma_unit<false>(wq + (size_t)wq_stage(0) * (64 * 8192), mine, oq);
  asm volatile("s_barrier" ::: "memory");
  dma_unit<false>(wq + (size_t)wq_stage(1) * (64 * 8192), mine + 32768, oq);
  dma_unit<false>(wq + (size_t)wq_stage(2) * (64 * 8192), mine + 65536, oq);
#pragma unroll
  for (int i = 0; i < 4; ++i) ok.v[i] = (r8 + 8 * i) * 512 + 16 * (ch ^ ((4 * i + (l >> 4)) & 7));

  const unsigned blk = (l >> 4) & 1, q4 = (l & 15) >> 2, p = l & 3, pp = ((p & 1) << 1) | (p >> 1);
  const unsigned trl = 32 * blk + 128 * q4;
  const unsigned trs = kRing + (unsigned)(w >> 1) * 1024 + trl + 64 * ((w & 1) ^ ((q4 >> 1) & 1));
  const unsigned xrow = (l & 31) * 128, xsw = 16 * (h ^ (((l & 31) >> 1) & 7));
  const unsigned blockA = kRegionA + (unsigned)w * 8192 + l * 16;

  f32x16 acc[2][2];
  Frag fr[2];
  zero16(acc[0][0]); zero16(acc[0][1]);

  auto read_q = [&](Frag& f, int u) {
    read_a<512>(f, trs + (u % 3) * 32768 + h * 4096 + 8 * pp);
    const unsigned xa = kRegionA + wq_stage(u) * 8192 + xrow;
#pragma unroll
    for (int s = 0; s < 4; ++s) {
      f.b[s][0] = lds_b128(xa + ((s * 32) ^ xsw));
      f.b[s][1] = lds_b128(xa + ((s * 32) ^ xsw) + 4096);
    }
  };
  vmwait<8>();
  BAR();
  STAMP(1);
  read_q(fr[0], 0);
#pragma unroll
  for (int u = 0; u < 7; ++u) {
    if (u < 6) vmwait<4>();
    BAR();
    if (u < 6) read_q(fr[(u + 1) & 1], u + 1);
    stage_mma(fr[u & 1], acc[0]);
    if (u <= 3) dma_unit<false>(wq + (size_t)wq_stage(u + 3) * (64 * 8192), mine + (u % 3) * 32768, oq);
    else dma_unit<NT_KV>(kc + ((u - 4) & 1) * (32 * 512) + ((u - 4) >> 1) * 128, mine + (u % 3) * 32768, ok);
  }
  STAMP(2);
#pragma unroll
  for (int s = 0; s < 2; ++s)
#pragma unroll
    for (int qb = 0; qb < 2; ++qb) sts_b128(blockA + (s * 2 + qb) * 1024, pack8(acc[0][qb], s));
  zero16(acc[0][0]); zero16(acc[0][1]); zero16(acc[1][0]); zero16(acc[1][1]);
#pragma unroll
  for (int i = 0; i < 4; ++i) ov.v[i] = r8 * 512 + ctr + i * 128;
  BAR();
  STAMP(3);

#pragma unroll
  for (int j = 0; j < 8; ++j) {
    const int dc = j >> 1, kb = j & 1;
    Frag& f = fr[dc & 1];
    if (kb == 0) {
#pragma unroll
      for (int s = 0; s < 4; ++s)
#pragma unroll
        for (int qb = 0; qb < 2; ++qb)
          f.b[s][qb] = lds_b128(kRegionA + (2 * dc + (s >> 1)) * 8192 + ((s & 1) * 2 + qb) * 1024 + l * 16);
    }
    vmwait<8>();
    const unsigned slot = mine + ((j + 1) % 3) * 32768;
#pragma unroll
    for (int s = 0; s < 4; ++s) f.a[s] = lds_b128(slot + xrow + ((s * 32) ^ xsw));
    stage_mma(f, acc[kb]);
    LGKM0();
    if (j <= 4) dma_unit<NT_KV>(kc + ((j + 3) & 1) * (32 * 512) + ((j + 3) >> 1) * 128, slot, ok);
    else if (j <= 6) dma_unit<NT_KV>(vc + (j - 5) * (64 * 512), slot, ov);
  }
  STAMP(4);

  Unit R[3];
  float lsum[2];
  {
    float mx[2];
#pragma unroll
    for (int qb = 0; qb < 2; ++qb) {
      float m = acc[0][qb][0];
#pragma unroll
      for (int kb = 0; kb < 2; ++kb)
#pragma unroll
        for (int r = 0; r < 16; ++r) m = fmaxf(m, acc[kb][qb][r]);
      mx[qb] = fmaxf(m, other_half(m));
    }
    sts_f1(kRegionA + w * 8192 + 4096 + l * 4, h ? mx[1] : mx[0]);
    BAR();
    float m = lds_f1(kRegionA + 4096 + l * 4);
#pragma unroll
    for (int i = 1; i < 8; ++i) m = fmaxf(m, lds_f1(kRegionA + i * 8192 + 4096 + l * 4));
    const float mo = other_half(m);
    const float sc = 0.0625f * 1.44269504088896340736f;
    const float m0 = (h ? mo : m) * sc, m1 = (h ? m : mo) * sc;
    BAR();
    dma_unit<NT_KV>(vc + 2 * (64 * 512), mine + 65536, ov);
#pragma unroll
    for (int kb = 0; kb < 2; ++kb) {
#pragma unroll
      for (int qb = 0; qb < 2; ++qb) {
        float sum = kb ? lsum[qb] : 0.f;
        const float mm = qb ? m1 : m0;
#pragma unroll
        for (int r = 0; r < 16; ++r) {
          const float e = __builtin_amdgcn_exp2f(fmaf(acc[kb][qb][r], sc, -mm));
          acc[kb][qb][r] = e;
          sum += e;
        }
        lsum[qb] = sum;
#pragma unroll
        for (int s = 0; s < 2; ++s) sts_b128(blockA + ((2 * kb + s) * 2 + qb) * 1024, pack8(acc[kb][qb], s));
      }
      get_unit<NT_KV>(R[kb], vc + (3 + kb) * (64 * 512), ov);
    }
    lsum[0] += other_half(lsum[0]);
    lsum[1] += other_half(lsum[1]);
    zero16(acc[0][0]); zero16(acc[0][1]);
  }
#pragma unroll
  for (int i = 0; i < 4; ++i) oo.v[i] = (r8 + 8 * i) * 896 + ctr;

  constexpr unsigned kStat = kRing + 2 * 32768 + 7 * 4096;
  auto read_v = [&](Frag& f, int j) {
    read_a<4096>(f, trs + (j % 3) * 32768 + h * 512 + 8 * p);
#pragma unroll
    for (int s = 0; s < 4; ++s)
#pragma unroll
      for (int qb = 0; qb < 2; ++qb) f.b[s][qb] = lds_b128(kRegionA + j * 8192 + (s * 2 + qb) * 1024 + l * 16);
  };
  auto wo_unit = [&](int j) { return wo + ((j + b) & 7) * (32 * 896); };
  auto wo_buf = [&](int j) {
    return (j & 3) == 0 ? mine + 65536 : (j & 3) == 1 ? mine : kRegionA + (unsigned)w * 8192 + ((j & 3) - 2) * 4096;
  };
  vmwait<16>();
  BAR();
  STAMP(5);
  read_v(fr[0], 0);
  get_unit<NT_KV>(R[2], vc + 5 * (64 * 512), ov);
#pragma unroll
  for (int j = 0; j < 8; ++j) {
    if (j == 0) vmwait<16>();
    BAR();
    if (j < 7) read_v(fr[(j + 1) & 1], j + 1);
    if (j == 5) sts_f1(kStat + w * 256 + l * 4, h ? lsum[1] : lsum[0]);
    if (j == 7 && ow) {
      dma_unit<false>(wo_unit(2), wo_buf(2), oo);
      dma_unit<false>(wo_unit(3), wo_buf(3), oo);
    }
    stage_mma(fr[j & 1], acc[0]);
    if (j == 0) { put_unit<8>(R[0], mine + l * 16); get_unit<NT_KV>(R[0], vc + 6 * (64 * 512), ov); }
    if (j == 1) { put_unit<8>(R[1], mine + 32768 + l * 16); get_unit<NT_KV>(R[1], vc + 7 * (64 * 512), ov); }
    if (j == 3) put_unit<8>(R[2], mine + 65536 + l * 16);
    if (j == 4) put_unit<4>(R[0], mine + l * 16);
    if (j == 5) {
      if (ow) dma_unit<false>(wo_unit(0), wo_buf(0), oo);
      else vmwait<0>();
      put_unit<4>(R[1], mine + 32768 + l * 16);
    }
    if (j == 6 && ow) dma_unit<false>(wo_unit(1), wo_buf(1), oo);
  }
  STAMP(6);
#pragma unroll
  for (int qb = 0; qb < 2; ++qb) {
    float tot = 0.f;
#pragma unroll
    for (int i = 0; i < 8; ++i) tot += lds_f1(kStat + i * 256 + (32 * qb + (l & 31)) * 4);
    const float inv = 1.0f / tot;
#pragma unroll
    for (int s = 0; s < 2; ++s) sts_b128(mine + 32768 + (s * 2 + qb) * 1024 + l * 16, pack8(acc[0][qb], s, inv));
  }
  zero16(acc[0][0]); zero16(acc[0][1]); zero16(acc[1][0]); zero16(acc[1][1]);
  BAR();
  STAMP(7);

  if (ow) {
    const unsigned tro = trl + 64 * ((q4 >> 1) & 1) + h * 512 + 8 * pp;
    auto read_o = [&](Frag& f, int j) {
      const unsigned uu = (j + b) & 7;
      const unsigned src = wo_buf(j) + tro;
#pragma unroll
      for (int s = 0; s < 2; ++s) {
#pragma unroll
        for (int cb = 0; cb < 2; ++cb) {
          const unsigned a = (cb ? (src ^ 64u) : src) + s * 2048;
          f.a[2 * s + cb] = cat(lds_tr(a), lds_tr(a + 1024));
        }
#pragma unroll
        for (int qb = 0; qb < 2; ++qb) f.b[s][qb] = lds_b128(kRing + 32768 + uu * 4096 + (s * 2 + qb) * 1024 + l * 16);
      }
    };
    vmwait<12>();
    read_o(fr[0], 0);
#pragma unroll
    for (int j = 0; j < 8; ++j) {
      if (j <= 4) vmwait<8>();
      if (j == 5) vmwait<4>();
      if (j == 6) vmwait<0>();
      if (j < 7) read_o(fr[(j + 1) & 1], j + 1);
      if (j <= 3) {
        lgkmwait<12>();
        dma_unit<false>(wo_unit(j + 4), wo_buf(j), oo);
      }
      const Frag& f = fr[j & 1];
#pragma unroll
      for (int s = 0; s < 2; ++s)
#pragma unroll
        for (int cb = 0; cb < 2; ++cb)
#pragma unroll
          for (int qb = 0; qb < 2; ++qb) acc[cb][qb] = mfma(f.a[2 * s + cb], f.b[s][qb], acc[cb][qb]);
    }
    STAMP(8);
    char* dst = part + (size_t)((br * 8 + b) * kHq + hq) * kPartBytes + w * 8192 + l * 16;
#pragma unroll
    for (int nb = 0; nb < 2; ++nb)
#pragma unroll
      for (int qb = 0; qb < 2; ++qb)
#pragma unroll
        for (int s = 0; s < 2; ++s) *(bf16x8*)(dst + ((nb * 2 + qb) * 2 + s) * 1024) = pack8(acc[nb][qb], s);
  }
}

__global__ __launch_bounds__(256) void head_reduce_kernel(const char* __restrict__ part, char* __restrict__ out) {
  const int t = blockIdx.x * 256 + threadIdx.x;
  const int g = t / 3584, ck = t % 3584;
  const char* src = part + (size_t)g * kHq * kPartBytes + ck * 16;
  u32x4 v[16];
#pragma unroll
  for (int i = 0; i < 16; ++i) v[i] = *(const u32x4*)(src + (size_t)i * kPartBytes);
  float s[8];
#pragma unroll
  for (int j = 0; j < 8; ++j) s[j] = 0.f;
#pragma unroll
  for (int i = 0; i < 16; ++i)
#pragma unroll
    for (int j = 0; j < 4; ++j) {
      s[2 * j] += __builtin_bit_cast(float, v[i][j] << 16);
      s[2 * j + 1] += __builtin_bit_cast(float, v[i][j] & 0xffff0000u);
    }
  bf16x8 r;
#pragma unroll
  for (int j = 0; j < 4; ++j) {
    f32x2 pr = {s[2 * j], s[2 * j + 1]};
    bf16x2 cv = __builtin_convertvector(pr, bf16x2);
    r[2 * j] = cv[0];
    r[2 * j + 1] = cv[1];
  }
  const int lane = ck & 63, fr = (ck >> 6) & 7, w = ck >> 9;
  const int nb = fr >> 2, qb = (fr >> 1) & 1, sh = fr & 1;
  const int q = 32 * qb + (lane & 31), n = 64 * w + 32 * nb + 16 * sh + 8 * (lane >> 5);
  const int br = g >> 3, b = g & 7;
  *(bf16x8*)(out + ((size_t)(b * kSq + q) * (2 * kD) + br * kD + n) * 2) = r;
}

extern "C" void kernel_launch(void* const* d_in, const int* in_sizes, int n_in, void* d_out, int out_size, void* d_ws,
                              size_t ws_size, hipStream_t stream) {
  (void)in_sizes; (void)n_in; (void)out_size; (void)ws_size;
  (void)hipFuncSetAttribute((const void*)gqa_fused_kernel, hipFuncAttributeMaxDynamicSharedMemorySize, kLdsBytes);
  gqa_fused_kernel<<<256, 512, kLdsBytes, stream>>>(
      (const char*)d_in[0], (const char*)d_in[1], (const char*)d_in[2], (const char*)d_in[3], (const char*)d_in[4],
      (const char*)d_in[5], (const char*)d_in[6], (const char*)d_in[7], (const char*)d_in[8], (const char*)d_in[9],
      (char*)d_ws);
  head_reduce_kernel<<<224, 256, 0, stream>>>((const char*)d_ws, (char*)d_out);
}
```

```cpp
#include <hip/hip_runtime.h>
#include <hip/hip_bf16.h>

typedef __attribute__((ext_vector_type(8))) __bf16 bf16x8;
typedef __attribute__((ext_vector_type(4))) __bf16 bf16x4;
typedef __attribute__((ext_vector_type(2))) __bf16 bf16x2;
typedef __attribute__((ext_vector_type(16))) float f32x16;
typedef __attribute__((ext_vector_type(4))) float f32x4;
typedef __attribute__((ext_vector_type(2))) float f32x2;
typedef __attribute__((ext_vector_type(4))) unsigned u32x4;
#define LDS_AS __attribute__((address_space(3)))
#define DI __device__ __forceinline__

#define NT_KV 1

constexpr int kD = 448, kHq = 16, kHkv = 8, kSk = 512, kDh = 256, kSq = 64;
constexpr unsigned kRegionA = 0;
constexpr unsigned kRing = 65536;
constexpr unsigned kLdsBytes = 163840;
constexpr int kPartBytes = 7 * 8192;
constexpr size_t kFlagOff = 16u << 20;
constexpr unsigned kSpinCap = 1u << 22;
typedef unsigned long long u64;

#define BAR() asm volatile("s_waitcnt lgkmcnt(0)\n\ts_barrier" ::: "memory")
#define LGKM0() asm volatile("s_waitcnt lgkmcnt(0)" ::: "memory")
#define VMW(n) asm volatile("s_waitcnt vmcnt(" #n ")" ::: "memory")
#define STAMP(i)

template <bool NT>
DI void dma4(const char* sbase, unsigned ldsdst, unsigned v0, unsigned v1, unsigned v2, unsigned v3) {
  unsigned keep;
  if (NT) {
    asm volatile(
        "s_mov_b32 %0, m0\n\ts_mov_b32 m0, %6\n\ts_nop 0\n\t"
        "global_load_lds_dwordx4 %1, %5 nt\n\ts_add_u32 m0, m0, 0x400\n\ts_nop 0\n\t"
        "global_load_lds_dwordx4 %2, %5 nt\n\ts_add_u32 m0, m0, 0x400\n\ts_nop 0\n\t"
        "global_load_lds_dwordx4 %3, %5 nt\n\ts_add_u32 m0, m0, 0x400\n\ts_nop 0\n\t"
        "global_load_lds_dwordx4 %4, %5 nt\n\ts_mov_b32 m0, %0"
        : "=&s"(keep)
        : "v"(v0), "v"(v1), "v"(v2), "v"(v3), "s"(sbase), "s"(ldsdst)
        : "memory", "scc");
  } else {
    asm volatile(
        "s_mov_b32 %0, m0\n\ts_mov_b32 m0, %6\n\ts_nop 0\n\t"
        "global_load_lds_dwordx4 %1, %5\n\ts_add_u32 m0, m0, 0x400\n\ts_nop 0\n\t"
        "global_load_lds_dwordx4 %2, %5\n\ts_add_u32 m0, m0, 0x400\n\ts_nop 0\n\t"
        "global_load_lds_dwordx4 %3, %5\n\ts_add_u32 m0, m0, 0x400\n\ts_nop 0\n\t"
        "global_load_lds_dwordx4 %4, %5\n\ts_mov_b32 m0, %0"
        : "=&s"(keep)
        : "v"(v0), "v"(v1), "v"(v2), "v"(v3), "s"(sbase), "s"(ldsdst)
        : "memory", "scc");
  }
}

DI void dma1(const char* sbase, unsigned ldsdst, unsigned v0) {
  unsigned keep;
  asm volatile("s_mov_b32 %0, m0\n\ts_mov_b32 m0, %3\n\ts_nop 0\n\tglobal_load_lds_dwordx4 %1, %2\n\ts_mov_b32 m0, %0"
               : "=&s"(keep)
               : "v"(v0), "s"(sbase), "s"(ldsdst)
               : "memory");
}

DI bf16x8 lds_b128(unsigned a) { return *(const LDS_AS bf16x8*)(size_t)a; }
DI f32x4 lds_f4(unsigned a) { return *(const LDS_AS f32x4*)(size_t)a; }
DI float lds_f1(unsigned a) { return *(const LDS_AS float*)(size_t)a; }
DI void sts_b128(unsigned a, bf16x8 v) { *(LDS_AS bf16x8*)(size_t)a = v; }
DI void sts_f4(unsigned a, f32x4 v) { *(LDS_AS f32x4*)(size_t)a = v; }
DI void sts_f1(unsigned a, float v) { *(LDS_AS float*)(size_t)a = v; }
DI bf16x4 lds_tr(unsigned a) { return __builtin_amdgcn_ds_read_tr16_b64_v4bf16((LDS_AS bf16x4*)(size_t)a); }
DI bf16x8 cat(bf16x4 a, bf16x4 b) { return __builtin_shufflevector(a, b, 0, 1, 2, 3, 4, 5, 6, 7); }
DI f32x16 mfma(bf16x8 a, bf16x8 b, f32x16 c) { return __builtin_amdgcn_mfma_f32_32x32x16_bf16(a, b, c, 0, 0, 0); }

DI bf16x8 pack8(const f32x16& t, int s, float scale) {
  bf16x8 r;
#pragma unroll
  for (int j = 0; j < 4; ++j) {
    f32x2 p = {t[8 * s + 2 * j] * scale, t[8 * s + 2 * j + 1] * scale};
    bf16x2 c = __builtin_convertvector(p, bf16x2);
    r[2 * j] = c[0];
    r[2 * j + 1] = c[1];
  }
  return r;
}
DI bf16x8 pack8(const f32x16& t, int s) {
  bf16x8 r;
#pragma unroll
  for (int j = 0; j < 4; ++j) {
    f32x2 p = {t[8 * s + 2 * j], t[8 * s + 2 * j + 1]};
    bf16x2 c = __builtin_convertvector(p, bf16x2);
    r[2 * j] = c[0];
    r[2 * j + 1] = c[1];
  }
  return r;
}
DI float other_half(float v) { return __shfl_xor(v, 32, 64); }

template <int N>
DI void vmwait() { asm volatile("s_waitcnt vmcnt(%0)" ::"n"(N) : "memory"); }

struct Unit { u32x4 r[4]; };
struct Off4 { unsigned v[4]; };

template <bool NT>
DI void get_unit(Unit& u, const char* sbase, const Off4& o) {
  if (NT) {
    asm volatile(
        "global_load_dwordx4 %0, %4, %8 nt\n\tglobal_load_dwordx4 %1, %5, %8 nt\n\t"
        "global_load_dwordx4 %2, %6, %8 nt\n\tglobal_load_dwordx4 %3, %7, %8 nt"
        : "=&v"(u.r[0]), "=&v"(u.r[1]), "=&v"(u.r[2]), "=&v"(u.r[3])
        : "v"(o.v[0]), "v"(o.v[1]), "v"(o.v[2]), "v"(o.v[3]), "s"(sbase)
        : "memory");
  } else {
    asm volatile(
        "global_load_dwordx4 %0, %4, %8\n\tglobal_load_dwordx4 %1, %5, %8\n\t"
        "global_load_dwordx4 %2, %6, %8\n\tglobal_load_dwordx4 %3, %7, %8"
        : "=&v"(u.r[0]), "=&v"(u.r[1]), "=&v"(u.r[2]), "=&v"(u.r[3])
        : "v"(o.v[0]), "v"(o.v[1]), "v"(o.v[2]), "v"(o.v[3]), "s"(sbase)
        : "memory");
  }
}
template <int N>
DI void put_unit(const Unit& u, unsigned addr) {
  asm volatile(
      "s_waitcnt vmcnt(%5)\n\tds_write_b128 %0, %1\n\tds_write_b128 %0, %2 offset:1024\n\t"
      "ds_write_b128 %0, %3 offset:2048\n\tds_write_b128 %0, %4 offset:3072"
      :
      : "v"(addr), "v"(u.r[0]), "v"(u.r[1]), "v"(u.r[2]), "v"(u.r[3]), "n"(N)
      : "memory");
}
template <bool NT>
DI void dma_unit(const char* sbase, unsigned dst, const Off4& o) { dma4<NT>(sbase, dst, o.v[0], o.v[1], o.v[2], o.v[3]); }

template <bool NT>
DI void dma_piece(const char* sbase, unsigned dst, unsigned v) {
  unsigned keep;
  if (NT) {
    asm volatile("s_mov_b32 %0, m0\n\ts_mov_b32 m0, %3\n\ts_nop 0\n\tglobal_load_lds_dwordx4 %1, %2 nt\n\ts_mov_b32 m0, %0"
                 : "=&s"(keep) : "v"(v), "s"(sbase), "s"(dst) : "memory");
  } else {
    asm volatile("s_mov_b32 %0, m0\n\ts_mov_b32 m0, %3\n\ts_nop 0\n\tglobal_load_lds_dwordx4 %1, %2\n\ts_mov_b32 m0, %0"
                 : "=&s"(keep) : "v"(v), "s"(sbase), "s"(dst) : "memory");
  }
}
template <bool NT>
DI void get_piece(u32x4& r, const char* sbase, unsigned v) {
  if (NT) asm volatile("global_load_dwordx4 %0, %1, %2 nt" : "=&v"(r) : "v"(v), "s"(sbase) : "memory");
  else asm volatile("global_load_dwordx4 %0, %1, %2" : "=&v"(r) : "v"(v), "s"(sbase) : "memory");
}
template <int OFF>
DI void put_piece(const u32x4& r, unsigned addr) {
  asm volatile("ds_write_b128 %0, %1 offset:%2" ::"v"(addr), "v"(r), "n"(OFF) : "memory");
}
#define SB() __builtin_amdgcn_sched_barrier(0)

DI void zero16(f32x16& a) {
#pragma unroll
  for (int r = 0; r < 16; ++r) a[r] = 0.f;
}

template <int N>
DI void lgkmwait() { asm volatile("s_waitcnt lgkmcnt(%0)" ::"n"(N) : "memory"); }

struct Frag { bf16x8 a[4], b[4][2]; };

template <int TOFF>
DI void read_a(Frag& f, unsigned abase) {
#pragma unroll
  for (int s = 0; s < 4; ++s) f.a[s] = cat(lds_tr(abase + s * 8192), lds_tr(abase + s * 8192 + TOFF));
}
DI void stage_mma(const Frag& f, f32x16 (&acc)[2]) {
#pragma unroll
  for (int s = 0; s < 4; ++s)
#pragma unroll
    for (int qb = 0; qb < 2; ++qb) acc[qb] = mfma(f.a[s], f.b[s][qb], acc[qb]);
}

__global__ __launch_bounds__(512, 2) void gqa_fused_kernel(
    const char* __restrict__ ax, const char* __restrict__ ak, const char* __restrict__ av, const char* __restrict__ awq,
    const char* __restrict__ awo, const char* __restrict__ bx, const char* __restrict__ bk, const char* __restrict__ bv,
    const char* __restrict__ bwq, const char* __restrict__ bwo, char* part, char* __restrict__ out) {
  const int bid = blockIdx.x, xcd = bid & 7, idx = bid >> 3;
  const int br = xcd >> 2, hq = (xcd & 3) * 4 + (idx & 3), b = idx >> 2, hkv = hq >> 1;
  const unsigned l = threadIdx.x & 63, h = l >> 5;
  const int w = __builtin_amdgcn_readfirstlane(threadIdx.x >> 6);
  STAMP(0);
  const char* wq = (br ? bwq : awq) + ((size_t)(8 * w) * 4096 + hq * 256) * 2;
  const char* kc = (br ? bk : ak) + ((size_t)((b * kHkv + hkv) * kSk + 64 * w) * kDh) * 2;
  const char* vc = (br ? bv : av) + ((size_t)((b * kHkv + hkv) * kSk + 8 * w) * kDh) * 2;
  const char* wo = (br ? bwo : awo) + ((size_t)(hq * 256) * kD + 64 * w) * 2;
  const unsigned mine = kRing + (unsigned)w * 4096;
  const unsigned r8 = l >> 3, ch = l & 7, ctr = 16 * (ch ^ (4 * ((l >> 4) & 1)));
  const int rq = b == 7 ? 0 : b;
  const bool ow = w < 7;

  {
    const char* xs = (br ? bx : ax) + (size_t)(b * kSq + 8 * w) * (kD * 2);
    const unsigned row = 8 * w + r8;
    const unsigned v = r8 * 896 + 16 * (ch ^ ((row >> 1) & 7));
#pragma unroll
    for (int j = 0; j < 7; ++j) dma1(xs + j * 128, kRegionA + j * 8192 + w * 1024, v);
  }
  Off4 oq, ok, ov, oo;
#pragma unroll
  for (int i = 0; i < 4; ++i) oq.v[i] = r8 * 8192 + ctr + i * 128;
  auto wq_stage = [&](int u) { int uu = u + rq; return uu >= 7 ? uu - 7 : uu; };
  dma_unit<false>(wq + (size_t)wq_stage(0) * (64 * 8192), mine, oq);
  asm volatile("s_barrier" ::: "memory");
  dma_unit<false>(wq + (size_t)wq_stage(1) * (64 * 8192), mine + 32768, oq);
  dma_unit<false>(wq + (size_t)wq_stage(2) * (64 * 8192), mine + 65536, oq);
#pragma unroll
  for (int i = 0; i < 4; ++i) ok.v[i] = (r8 + 8 * i) * 512 + 16 * (ch ^ ((4 * i + (l >> 4)) & 7));

  const unsigned blk = (l >> 4) & 1, q4 = (l & 15) >> 2, p = l & 3, pp = ((p & 1) << 1) | (p >> 1);
  const unsigned trl = 32 * blk + 128 * q4;
  const unsigned trs = kRing + (unsigned)(w >> 1) * 1024 + trl + 64 * ((w & 1) ^ ((q4 >> 1) & 1));
  const unsigned xrow = (l & 31) * 128, xsw = 16 * (h ^ (((l & 31) >> 1) & 7));
  const unsigned blockA = kRegionA + (unsigned)w * 8192 + l * 16;

  f32x16 acc[2][2];
  Frag fr[2];
  zero16(acc[0][0]); zero16(acc[0][1]);

  auto read_q = [&](Frag& f, int u, int s) {
    const unsigned aa = trs + (u % 3) * 32768 + h * 4096 + 8 * pp + s * 8192;
    f.a[s] = cat(lds_tr(aa), lds_tr(aa + 512));
    const unsigned xa = kRegionA + wq_stage(u) * 8192 + xrow + ((s * 32) ^ xsw);
    f.b[s][0] = lds_b128(xa);
    f.b[s][1] = lds_b128(xa + 4096);
  };
  vmwait<8>();
  BAR();
  STAMP(1);
#pragma unroll
  for (int s = 0; s < 4; ++s) read_q(fr[0], 0, s);
#pragma unroll
  for (int u = 0; u < 7; ++u) {
    if (u < 6) vmwait<4>();
    BAR();
    STAMP(2 + u);
    const char* src = u <= 3 ? wq + (size_t)wq_stage(u + 3) * (64 * 8192) : kc + ((u - 4) & 1) * (32 * 512) + ((u - 4) >> 1) * 128;
#pragma unroll
    for (int i = 0; i < 8; ++i) {
      acc[0][i & 1] = mfma(fr[u & 1].a[i >> 1], fr[u & 1].b[i >> 1][i & 1], acc[0][i & 1]);
      SB();
      if (u < 6 && i < 4) read_q(fr[(u + 1) & 1], u + 1, i);
      SB();
      if (!(i & 1)) {
        if (u <= 3) dma_piece<false>(src, mine + (u % 3) * 32768 + (i >> 1) * 1024, oq.v[i >> 1]);
        else dma_piece<NT_KV>(src, mine + (u % 3) * 32768 + (i >> 1) * 1024, ok.v[i >> 1]);
      }
      SB();
    }
  }
  STAMP(9);
#pragma unroll
  for (int s = 0; s < 2; ++s)
#pragma unroll
    for (int qb = 0; qb < 2; ++qb) sts_b128(blockA + (s * 2 + qb) * 1024, pack8(acc[0][qb], s));
  zero16(acc[0][0]); zero16(acc[0][1]); zero16(acc[1][0]); zero16(acc[1][1]);
#pragma unroll
  for (int i = 0; i < 4; ++i) ov.v[i] = r8 * 512 + ctr + i * 128;
  BAR();
  STAMP(10);

#pragma unroll
  for (int j = 0; j < 8; ++j) {
    const int dc = j >> 1, kb = j & 1;
    Frag& f = fr[dc & 1];
    if (kb == 0) {
#pragma unroll
      for (int s = 0; s < 4; ++s)
#pragma unroll
        for (int qb = 0; qb < 2; ++qb)
          f.b[s][qb] = lds_b128(kRegionA + (2 * dc + (s >> 1)) * 8192 + ((s & 1) * 2 + qb) * 1024 + l * 16);
    }
    vmwait<8>();
    if (j & 1) STAMP(11 + (j >> 1));
    const unsigned slot = mine + ((j + 1) % 3) * 32768;
#pragma unroll
    for (int s = 0; s < 4; ++s) f.a[s] = lds_b128(slot + xrow + ((s * 32) ^ xsw));
    const char* src = j <= 4 ? kc + ((j + 3) & 1) * (32 * 512) + ((j + 3) >> 1) * 128 : vc + (j - 5) * (64 * 512);
#pragma unroll
    for (int i = 0; i < 8; ++i) {
      acc[kb][i & 1] = mfma(f.a[i >> 1], f.b[i >> 1][i & 1], acc[kb][i & 1]);
      SB();
      if (i == 1) LGKM0();
      if ((i & 1) && j <= 6) dma_piece<NT_KV>(src, slot + (i >> 1) * 1024, j <= 4 ? ok.v[i >> 1] : ov.v[i >> 1]);
      SB();
    }
  }
  STAMP(15);

  Unit R[3];
  float lsum[2];
  {
    float mx[2];
#pragma unroll
    for (int qb = 0; qb < 2; ++qb) {
      float m = acc[0][qb][0];
#pragma unroll
      for (int kb = 0; kb < 2; ++kb)
#pragma unroll
        for (int r = 0; r < 16; ++r) m = fmaxf(m, acc[kb][qb][r]);
      mx[qb] = fmaxf(m, other_half(m));
    }
    sts_f1(kRegionA + w * 8192 + 4096 + l * 4, h ? mx[1] : mx[0]);
    BAR();
    STAMP(16);
    float m = lds_f1(kRegionA + 4096 + l * 4);
#pragma unroll
    for (int i = 1; i < 8; ++i) m = fmaxf(m, lds_f1(kRegionA + i * 8192 + 4096 + l * 4));
    const float mo = other_half(m);
    const float sc = 0.0625f * 1.44269504088896340736f;
    const float m0 = (h ? mo : m) * sc, m1 = (h ? m : mo) * sc;
    BAR();
    dma_unit<NT_KV>(vc + 2 * (64 * 512), mine + 65536, ov);
#pragma unroll
    for (int kb = 0; kb < 2; ++kb) {
#pragma unroll
      for (int qb = 0; qb < 2; ++qb) {
        float sum = kb ? lsum[qb] : 0.f;
        const float mm = qb ? m1 : m0;
#pragma unroll
        for (int r = 0; r < 16; ++r) {
          const float e = __builtin_amdgcn_exp2f(fmaf(acc[kb][qb][r], sc, -mm));
          acc[kb][qb][r] = e;
          sum += e;
        }
        lsum[qb] = sum;
#pragma unroll
        for (int s = 0; s < 2; ++s) sts_b128(blockA + ((2 * kb + s) * 2 + qb) * 1024, pack8(acc[kb][qb], s));
      }
      get_unit<NT_KV>(R[kb], vc + (3 + kb) * (64 * 512), ov);
    }
    lsum[0] += other_half(lsum[0]);
    lsum[1] += other_half(lsum[1]);
    zero16(acc[0][0]); zero16(acc[0][1]);
  }
#pragma unroll
  for (int i = 0; i < 4; ++i) oo.v[i] = (r8 + 8 * i) * 896 + ctr;

  constexpr unsigned kStat = kRing + 2 * 32768 + 7 * 4096;
  auto read_v = [&](Frag& f, int j, int s) {
    const unsigned aa = trs + (j % 3) * 32768 + h * 512 + 8 * p + s * 8192;
    f.a[s] = cat(lds_tr(aa), lds_tr(aa + 4096));
#pragma unroll
    for (int qb = 0; qb < 2; ++qb) f.b[s][qb] = lds_b128(kRegionA + j * 8192 + (s * 2 + qb) * 1024 + l * 16);
  };
  auto wo_unit = [&](int j) { return wo + ((j + b) & 7) * (32 * 896); };
  auto wo_buf = [&](int j) {
    return (j & 3) == 0 ? mine + 65536 : (j & 3) == 1 ? mine : kRegionA + (unsigned)w * 8192 + ((j & 3) - 2) * 4096;
  };
  vmwait<16>();
  BAR();
  STAMP(17);
#pragma unroll
  for (int s = 0; s < 4; ++s) read_v(fr[0], 0, s);
  get_unit<NT_KV>(R[2], vc + 5 * (64 * 512), ov);
  const unsigned putb = mine + l * 16;
  auto put = [&](const Unit& u, unsigned a, int i) {
    if (i == 0) put_piece<0>(u.r[0], a);
    if (i == 1) put_piece<1024>(u.r[1], a);
    if (i == 2) put_piece<2048>(u.r[2], a);
    if (i == 3) put_piece<3072>(u.r[3], a);
  };
#pragma unroll
  for (int j = 0; j < 8; ++j) {
    if (j == 0) vmwait<16>();
    BAR();
    STAMP(18 + j);
    if (j == 5) sts_f1(kStat + w * 256 + l * 4, h ? lsum[1] : lsum[0]);
#pragma unroll
    for (int i = 0; i < 8; ++i) {
      acc[0][i & 1] = mfma(fr[j & 1].a[i >> 1], fr[j & 1].b[i >> 1][i & 1], acc[0][i & 1]);
      SB();
      if (j < 7 && i < 4) read_v(fr[(j + 1) & 1], j + 1, i);
      SB();
      const int k = i - 3;
      if (i == 3) {
        if (j == 0 || j == 1 || j == 3) vmwait<8>();
        if (j == 4) vmwait<4>();
        if (j == 5) vmwait<0>();
      }
      if (i >= 3 && i < 7) {
        if (j == 0) { put(R[0], putb, k); get_piece<NT_KV>(R[0].r[k], vc + 6 * (64 * 512), ov.v[k]); }
        if (j == 1) { put(R[1], putb + 32768, k); get_piece<NT_KV>(R[1].r[k], vc + 7 * (64 * 512), ov.v[k]); }
        if (j == 3) put(R[2], putb + 65536, k);
        if (j == 4) put(R[0], putb, k);
        if (j == 5) {
          put(R[1], putb + 32768, k);
          if (ow) dma_piece<false>(wo_unit(0), wo_buf(0) + k * 1024, oo.v[k]);
        }
      }
      if (j == 6 && ow && !(i & 1)) dma_piece<false>(wo_unit(1), wo_buf(1) + (i >> 1) * 1024, oo.v[i >> 1]);
      if (j == 7 && ow) dma_piece<false>(wo_unit(2 + (i >> 2)), wo_buf(2 + (i >> 2)) + (i & 3) * 1024, oo.v[i & 3]);
      SB();
    }
  }
  STAMP(26);
#pragma unroll
  for (int qb = 0; qb < 2; ++qb) {
    float tot = 0.f;
#pragma unroll
    for (int i = 0; i < 8; ++i) tot += lds_f1(kStat + i * 256 + (32 * qb + (l & 31)) * 4);
    const float inv = 1.0f / tot;
#pragma unroll
    for (int s = 0; s < 2; ++s) sts_b128(mine + 32768 + (s * 2 + qb) * 1024 + l * 16, pack8(acc[0][qb], s, inv));
  }
  zero16(acc[0][0]); zero16(acc[0][1]); zero16(acc[1][0]); zero16(acc[1][1]);
  BAR();
  STAMP(27);

  if (ow) {
    const unsigned tro = trl + 64 * ((q4 >> 1) & 1) + h * 512 + 8 * pp;
    auto read_o = [&](Frag& f, int j) {
      const unsigned uu = (j + b) & 7;
      const unsigned src = wo_buf(j) + tro;
#pragma unroll
      for (int s = 0; s < 2; ++s) {
#pragma unroll
        for (int cb = 0; cb < 2; ++cb) {
          const unsigned a = (cb ? (src ^ 64u) : src) + s * 2048;
          f.a[2 * s + cb] = cat(lds_tr(a), lds_tr(a + 1024));
        }
#pragma unroll
        for (int qb = 0; qb < 2; ++qb) f.b[s][qb] = lds_b128(kRing + 32768 + uu * 4096 + (s * 2 + qb) * 1024 + l * 16);
      }
    };
    vmwait<12>();
    read_o(fr[0], 0);
#pragma unroll
    for (int j = 0; j < 8; ++j) {
      if (j <= 4) vmwait<8>();
      if (j == 5) vmwait<4>();
      if (j == 6) vmwait<0>();
      if (j < 7) read_o(fr[(j + 1) & 1], j + 1);
      if (j <= 3) {
        lgkmwait<12>();
        dma_unit<false>(wo_unit(j + 4), wo_buf(j), oo);
      }
      const Frag& f = fr[j & 1];
#pragma unroll
      for (int s = 0; s < 2; ++s)
#pragma unroll
        for (int cb = 0; cb < 2; ++cb)
#pragma unroll
          for (int qb = 0; qb < 2; ++qb) acc[cb][qb] = mfma(f.a[2 * s + cb], f.b[s][qb], acc[cb][qb]);
    }
    STAMP(28);
    const char* dst = part + (size_t)((br * 8 + b) * kHq + hq) * kPartBytes + w * 8192;
    const unsigned so = (l & 31) * 32 + h * 16;
#pragma unroll
    for (int nb = 0; nb < 2; ++nb)
#pragma unroll
      for (int qb = 0; qb < 2; ++qb)
#pragma unroll
        for (int s = 0; s < 2; ++s)
          asm volatile("global_store_dwordx4 %0, %1, %2 sc1\n\ts_nop 1" ::"v"(so + ((nb * 2 + qb) * 2 + s) * 1024),
                       "v"(pack8(acc[nb][qb], s)), "s"(dst) : "memory");
    VMW(0);
    STAMP(29);
    asm volatile("s_barrier\n\ts_barrier" ::: "memory");
    STAMP(30);

    const char* src = part + (size_t)((br * 8 + b) * kHq) * kPartBytes + w * 8192 + hq * 512;
    const unsigned lo = h * (8 * kPartBytes) + (l & 31) * 16;
    u32x4 v[8];
    asm volatile(
        "global_load_dwordx4 %0, %8, %16 sc1\n\tglobal_load_dwordx4 %1, %9, %16 sc1\n\t"
        "global_load_dwordx4 %2, %10, %16 sc1\n\tglobal_load_dwordx4 %3, %11, %16 sc1\n\t"
        "global_load_dwordx4 %4, %12, %16 sc1\n\tglobal_load_dwordx4 %5, %13, %16 sc1\n\t"
        "global_load_dwordx4 %6, %14, %16 sc1\n\tglobal_load_dwordx4 %7, %15, %16 sc1\n\t"
        "s_waitcnt vmcnt(0)"
        : "=&v"(v[0]), "=&v"(v[1]), "=&v"(v[2]), "=&v"(v[3]), "=&v"(v[4]), "=&v"(v[5]), "=&v"(v[6]), "=&v"(v[7])
        : "v"(lo), "v"(lo + kPartBytes), "v"(lo + 2 * kPartBytes), "v"(lo + 3 * kPartBytes), "v"(lo + 4 * kPartBytes),
          "v"(lo + 5 * kPartBytes), "v"(lo + 6 * kPartBytes), "v"(lo + 7 * kPartBytes), "s"(src)
        : "memory");
    float sum[8];
#pragma unroll
    for (int j = 0; j < 8; ++j) sum[j] = 0.f;
#pragma unroll
    for (int i = 0; i < 8; ++i)
#pragma unroll
      for (int j = 0; j < 4; ++j) {
        sum[2 * j] += __builtin_bit_cast(float, v[i][j] << 16);
        sum[2 * j + 1] += __builtin_bit_cast(float, v[i][j] & 0xffff0000u);
      }
    bf16x4 r;
#pragma unroll
    for (int j = 0; j < 2; ++j) {
      const float k0 = h ? sum[4 + 2 * j] : sum[2 * j], k1 = h ? sum[5 + 2 * j] : sum[1 + 2 * j];
      const float g0 = h ? sum[2 * j] : sum[4 + 2 * j], g1 = h ? sum[1 + 2 * j] : sum[5 + 2 * j];
      f32x2 pr = {k0 + other_half(g0), k1 + other_half(g1)};
      bf16x2 cv = __builtin_convertvector(pr, bf16x2);
      r[2 * j] = cv[0];
      r[2 * j + 1] = cv[1];
    }
    const int fi = hq >> 1, t = l & 31;
    const int q = 32 * ((fi >> 1) & 1) + 16 * (hq & 1) + (t >> 1);
    const int n = 64 * w + 32 * (fi >> 2) + 16 * (fi & 1) + 8 * (t & 1) + 4 * h;
    *(bf16x4*)(out + ((size_t)(b * kSq + q) * (2 * kD) + br * kD + n) * 2) = r;
  } else {
    u64* flags = (u64*)(part + kFlagOff) + (br * 8 + b) * 32;
    const u64 tag = __hip_atomic_load(flags + 16, __ATOMIC_RELAXED, __HIP_MEMORY_SCOPE_AGENT) + 1;
    asm volatile("s_waitcnt vmcnt(0)\n\ts_barrier" ::: "memory");
    if (l == 0) __hip_atomic_store(flags + hq, tag, __ATOMIC_RELAXED, __HIP_MEMORY_SCOPE_AGENT);
    for (unsigned spin = 0; spin < kSpinCap; ++spin) {
      const u64 f = __hip_atomic_load(flags + (l & 15), __ATOMIC_RELAXED, __HIP_MEMORY_SCOPE_AGENT);
      if (__all(f == tag)) break;
      __builtin_amdgcn_s_sleep(1);
    }
    asm volatile("s_barrier" ::: "memory");
    if (hq == 0 && l == 0) __hip_atomic_store(flags + 16, tag, __ATOMIC_RELAXED, __HIP_MEMORY_SCOPE_AGENT);
  }
}

extern "C" void kernel_launch(void* const* d_in, const int* in_sizes, int n_in, void* d_out, int out_size, void* d_ws,
                              size_t ws_size, hipStream_t stream) {
  (void)in_sizes; (void)n_in; (void)out_size; (void)ws_size;
  (void)hipFuncSetAttribute((const void*)gqa_fused_kernel, hipFuncAttributeMaxDynamicSharedMemorySize, kLdsBytes);
  gqa_fused_kernel<<<256, 512, kLdsBytes, stream>>>(
      (const char*)d_in[0], (const char*)d_in[1], (const char*)d_in[2], (const char*)d_in[3], (const char*)d_in[4],
      (const char*)d_in[5], (const char*)d_in[6], (const char*)d_in[7], (const char*)d_in[8], (const char*)d_in[9],
      (char*)d_ws, (char*)d_out);
}
```

```cpp
#include <hip/hip_runtime.h>
#include <hip/hip_bf16.h>


typedef __attribute__((ext_vector_type(8))) __bf16 bf16x8;
typedef __attribute__((ext_vector_type(4))) __bf16 bf16x4;
typedef __attribute__((ext_vector_type(2))) __bf16 bf16x2;
typedef __attribute__((ext_vector_type(16))) float f32x16;
typedef __attribute__((ext_vector_type(2))) float f32x2;
typedef __attribute__((ext_vector_type(4))) unsigned u32x4;
#define LDS_AS __attribute__((address_space(3)))
#define DI __device__ __forceinline__

constexpr int kD = 448, kHq = 16, kHkv = 8, kSk = 512, kDh = 256, kSq = 64;
constexpr unsigned kRegionA = 0;
constexpr unsigned kRing = 65536;
constexpr unsigned kLdsBytes = 163840;
constexpr int kPartBytes = 7 * 8192;
constexpr size_t kFlagOff = 16u << 20;
constexpr unsigned kSpinCap = 1u << 20;
constexpr bool kNtKV = true;
typedef unsigned long long u64;

#define BAR() asm volatile("s_waitcnt lgkmcnt(0)\n\ts_barrier" ::: "memory")
#define BARN(n) asm volatile("s_waitcnt lgkmcnt(" #n ")\n\ts_barrier" ::: "memory")
#define LGKM0() asm volatile("s_waitcnt lgkmcnt(0)" ::: "memory")
#define VMW(n) asm volatile("s_waitcnt vmcnt(" #n ")" ::: "memory")

template <bool NT>
DI void dma4(const char* sbase, unsigned ldsdst, unsigned v0, unsigned v1, unsigned v2, unsigned v3) {
  unsigned keep;
  if (NT) {
    asm volatile(
        "s_mov_b32 %0, m0\n\ts_mov_b32 m0, %6\n\ts_nop 0\n\t"
        "global_load_lds_dwordx4 %1, %5 nt\n\ts_add_u32 m0, m0, 0x400\n\ts_nop 0\n\t"
        "global_load_lds_dwordx4 %2, %5 nt\n\ts_add_u32 m0, m0, 0x400\n\ts_nop 0\n\t"
        "global_load_lds_dwordx4 %3, %5 nt\n\ts_add_u32 m0, m0, 0x400\n\ts_nop 0\n\t"
        "global_load_lds_dwordx4 %4, %5 nt\n\ts_mov_b32 m0, %0"
        : "=&s"(keep)
        : "v"(v0), "v"(v1), "v"(v2), "v"(v3), "s"(sbase), "s"(ldsdst)
        : "memory", "scc");
  } else {
    asm volatile(
        "s_mov_b32 %0, m0\n\ts_mov_b32 m0, %6\n\ts_nop 0\n\t"
        "global_load_lds_dwordx4 %1, %5\n\ts_add_u32 m0, m0, 0x400\n\ts_nop 0\n\t"
        "global_load_lds_dwordx4 %2, %5\n\ts_add_u32 m0, m0, 0x400\n\ts_nop 0\n\t"
        "global_load_lds_dwordx4 %3, %5\n\ts_add_u32 m0, m0, 0x400\n\ts_nop 0\n\t"
        "global_load_lds_dwordx4 %4, %5\n\ts_mov_b32 m0, %0"
        : "=&s"(keep)
        : "v"(v0), "v"(v1), "v"(v2), "v"(v3), "s"(sbase), "s"(ldsdst)
        : "memory", "scc");
  }
}

DI bf16x8 lds_b128(unsigned a) { return *(const LDS_AS bf16x8*)(size_t)a; }
DI float lds_f1(unsigned a) { return *(const LDS_AS float*)(size_t)a; }
DI void sts_b128(unsigned a, bf16x8 v) { *(LDS_AS bf16x8*)(size_t)a = v; }
DI void sts_f1(unsigned a, float v) { *(LDS_AS float*)(size_t)a = v; }
DI bf16x4 lds_tr(unsigned a) { return __builtin_amdgcn_ds_read_tr16_b64_v4bf16((LDS_AS bf16x4*)(size_t)a); }
DI bf16x8 cat(bf16x4 a, bf16x4 b) { return __builtin_shufflevector(a, b, 0, 1, 2, 3, 4, 5, 6, 7); }
DI f32x16 mfma(bf16x8 a, bf16x8 b, f32x16 c) { return __builtin_amdgcn_mfma_f32_32x32x16_bf16(a, b, c, 0, 0, 0); }

DI bf16x8 pack8(const f32x16& t, int s, float scale) {
  bf16x8 r;
#pragma unroll
  for (int j = 0; j < 4; ++j) {
    f32x2 p = {t[8 * s + 2 * j] * scale, t[8 * s + 2 * j + 1] * scale};
    bf16x2 c = __builtin_convertvector(p, bf16x2);
    r[2 * j] = c[0];
    r[2 * j + 1] = c[1];
  }
  return r;
}
DI bf16x8 pack8(const f32x16& t, int s) {
  bf16x8 r;
#pragma unroll
  for (int j = 0; j < 4; ++j) {
    f32x2 p = {t[8 * s + 2 * j], t[8 * s + 2 * j + 1]};
    bf16x2 c = __builtin_convertvector(p, bf16x2);
    r[2 * j] = c[0];
    r[2 * j + 1] = c[1];
  }
  return r;
}
DI float other_half(float v) { return __shfl_xor(v, 32, 64); }

template <int N>
DI void vmwait() { asm volatile("s_waitcnt vmcnt(%0)" ::"n"(N) : "memory"); }

struct Unit { u32x4 r[4]; };
struct Off4 { unsigned v[4]; };

template <bool NT>
DI void get_unit(Unit& u, const char* sbase, const Off4& o) {
  if (NT) {
    asm volatile(
        "global_load_dwordx4 %0, %4, %8 nt\n\tglobal_load_dwordx4 %1, %5, %8 nt\n\t"
        "global_load_dwordx4 %2, %6, %8 nt\n\tglobal_load_dwordx4 %3, %7, %8 nt"
        : "=&v"(u.r[0]), "=&v"(u.r[1]), "=&v"(u.r[2]), "=&v"(u.r[3])
        : "v"(o.v[0]), "v"(o.v[1]), "v"(o.v[2]), "v"(o.v[3]), "s"(sbase)
        : "memory");
  } else {
    asm volatile(
        "global_load_dwordx4 %0, %4, %8\n\tglobal_load_dwordx4 %1, %5, %8\n\t"
        "global_load_dwordx4 %2, %6, %8\n\tglobal_load_dwordx4 %3, %7, %8"
        : "=&v"(u.r[0]), "=&v"(u.r[1]), "=&v"(u.r[2]), "=&v"(u.r[3])
        : "v"(o.v[0]), "v"(o.v[1]), "v"(o.v[2]), "v"(o.v[3]), "s"(sbase)
        : "memory");
  }
}
template <bool NT>
DI void dma_unit(const char* sbase, unsigned dst, const Off4& o) { dma4<NT>(sbase, dst, o.v[0], o.v[1], o.v[2], o.v[3]); }

template <bool NT>
DI void dma_piece(const char* sbase, unsigned dst, unsigned v) {
  unsigned keep;
  if (NT) {
    asm volatile("s_mov_b32 %0, m0\n\ts_mov_b32 m0, %3\n\ts_nop 0\n\tglobal_load_lds_dwordx4 %1, %2 nt\n\ts_mov_b32 m0, %0"
                 : "=&s"(keep) : "v"(v), "s"(sbase), "s"(dst) : "memory");
  } else {
    asm volatile("s_mov_b32 %0, m0\n\ts_mov_b32 m0, %3\n\ts_nop 0\n\tglobal_load_lds_dwordx4 %1, %2\n\ts_mov_b32 m0, %0"
                 : "=&s"(keep) : "v"(v), "s"(sbase), "s"(dst) : "memory");
  }
}
template <bool NT>
DI void get_piece(u32x4& r, const char* sbase, unsigned v) {
  if (NT) asm volatile("global_load_dwordx4 %0, %1, %2 nt" : "=&v"(r) : "v"(v), "s"(sbase) : "memory");
  else asm volatile("global_load_dwordx4 %0, %1, %2" : "=&v"(r) : "v"(v), "s"(sbase) : "memory");
}
template <int OFF>
DI void put_piece(const u32x4& r, unsigned addr) {
  asm volatile("ds_write_b128 %0, %1 offset:%2" ::"v"(addr), "v"(r), "n"(OFF) : "memory");
}
#define SB() __builtin_amdgcn_sched_barrier(0)

DI void zero16(f32x16& a) {
#pragma unroll
  for (int r = 0; r < 16; ++r) a[r] = 0.f;
}

struct Frag { bf16x8 a[4], b[4][2]; };

__global__ __launch_bounds__(512, 2) void gqa_fused_kernel(
    const char* __restrict__ ax, const char* __restrict__ ak, const char* __restrict__ av, const char* __restrict__ awq,
    const char* __restrict__ awo, const char* __restrict__ bx, const char* __restrict__ bk, const char* __restrict__ bv,
    const char* __restrict__ bwq, const char* __restrict__ bwo, char* part, char* __restrict__ out) {
  const int bid = blockIdx.x, xcd = bid & 7, idx = bid >> 3;
  const int br = xcd >> 2, hq = (xcd & 3) * 4 + (idx & 3), b = idx >> 2, hkv = hq >> 1;
  const unsigned l = threadIdx.x & 63, h = l >> 5;
  const int w = __builtin_amdgcn_readfirstlane(threadIdx.x >> 6);
  const char* wq = (br ? bwq : awq) + ((size_t)(8 * w) * 4096 + hq * 256) * 2;
  const char* kc = (br ? bk : ak) + ((size_t)((b * kHkv + hkv) * kSk + 64 * w) * kDh) * 2;
  const char* vc = (br ? bv : av) + ((size_t)((b * kHkv + hkv) * kSk + 8 * w) * kDh) * 2;
  const char* wo = (br ? bwo : awo) + ((size_t)(hq * 256) * kD + 64 * w) * 2;
  const unsigned mine = kRing + (unsigned)w * 4096;
  const unsigned r8 = l >> 3, ch = l & 7, ctr = 16 * (ch ^ (4 * ((l >> 4) & 1)));
  const int rq = b == 7 ? 0 : b;
  const bool ow = w < 7;

  const char* xs = (br ? bx : ax) + (size_t)(b * kSq + 8 * w) * (kD * 2);
  const unsigned ox = r8 * 896 + 16 * (ch ^ (((8 * w + r8) >> 1) & 7));
  Off4 oq, ok, ov, oo;
#pragma unroll
  for (int i = 0; i < 4; ++i) oq.v[i] = r8 * 8192 + ctr + i * 128;
  auto wq_stage = [&](int u) { int uu = u + rq; return uu >= 7 ? uu - 7 : uu; };
  auto x_piece = [&](int u) { dma_piece<false>(xs + wq_stage(u) * 128, kRegionA + u * 8192 + w * 1024, ox); };
  x_piece(0);
  dma_unit<false>(wq + (size_t)wq_stage(0) * (64 * 8192), mine, oq);
  asm volatile("s_barrier" ::: "memory");
  x_piece(1);
  dma_unit<false>(wq + (size_t)wq_stage(1) * (64 * 8192), mine + 32768, oq);
  x_piece(2);
  dma_unit<false>(wq + (size_t)wq_stage(2) * (64 * 8192), mine + 65536, oq);
#pragma unroll
  for (int i = 0; i < 4; ++i) ok.v[i] = (r8 + 8 * i) * 512 + 16 * (ch ^ ((4 * i + (l >> 4)) & 7));
  auto k_unit = [&](int j) { return kc + (j & 1) * (32 * 512) + (j >> 1) * 128; };
  auto v_unit = [&](int j) { return vc + j * (64 * 512); };

  const unsigned blk = (l >> 4) & 1, q4 = (l & 15) >> 2, p = l & 3, pp = ((p & 1) << 1) | (p >> 1);
  const unsigned trl = 32 * blk + 128 * q4;
  const unsigned trs = kRing + (unsigned)(w >> 1) * 1024 + trl + 64 * ((w & 1) ^ ((q4 >> 1) & 1));
  const unsigned xrow = (l & 31) * 128, xsw = 16 * (h ^ (((l & 31) >> 1) & 7));
  const unsigned blockA = kRegionA + (unsigned)w * 8192 + l * 16;

  f32x16 acc[2][2];
  Frag fr[2];
  zero16(acc[0][0]); zero16(acc[0][1]);

  Unit R[3];
  const unsigned putb = mine + l * 16;
  auto put = [&](const Unit& u, unsigned a, int i) {
    if (i == 0) put_piece<0>(u.r[0], a);
    if (i == 1) put_piece<1024>(u.r[1], a);
    if (i == 2) put_piece<2048>(u.r[2], a);
    if (i == 3) put_piece<3072>(u.r[3], a);
  };
  auto read_q = [&](Frag& f, int u, int i) {
    if (i < 4) {
      const unsigned aa = trs + (u % 3) * 32768 + h * 4096 + 8 * pp + i * 8192;
      f.a[i] = cat(lds_tr(aa), lds_tr(aa + 512));
    }
    f.b[i >> 1][i & 1] = lds_b128(kRegionA + u * 8192 + xrow + (((i >> 1) * 32) ^ xsw) + (i & 1) * 4096);
  };
  vmwait<10>();
  BAR();
#pragma unroll
  for (int i = 0; i < 8; ++i) read_q(fr[0], 0, i);
#pragma unroll
  for (int u = 0; u < 7; ++u) {
    if (u == 0) vmwait<5>();
    if (u == 1 || u == 4) vmwait<9>();
    if (u == 2 || u == 3) vmwait<13>();
    if (u == 5) vmwait<4>();
    if (u == 0 || u == 6) BAR();
    else BARN(4);
    const char* src = wq + (size_t)wq_stage(u + 3) * (64 * 8192);
#pragma unroll
    for (int i = 0; i < 8; ++i) {
      acc[0][i & 1] = mfma(fr[u & 1].a[i >> 1], fr[u & 1].b[i >> 1][i & 1], acc[0][i & 1]);
      SB();
      if (u < 6) read_q(fr[(u + 1) & 1], u + 1, i);
      SB();
      if (u <= 2) {
        if (i == 0) x_piece(u + 3);
        if (i < 4) dma_piece<false>(src, mine + (u % 3) * 32768 + i * 1024, oq.v[i]);
        else get_piece<kNtKV>(R[u].r[i - 4], k_unit(u + 3), ok.v[i - 4]);
      } else if (u == 3) {
        if (i == 1) x_piece(u + 3);
        if (!(i & 1)) dma_piece<false>(src, mine + (u % 3) * 32768 + (i >> 1) * 1024, oq.v[i >> 1]);
      } else if (!(i & 1)) {
        dma_piece<kNtKV>(k_unit(u - 4), mine + (u % 3) * 32768 + (i >> 1) * 1024, ok.v[i >> 1]);
      }
      SB();
    }
  }
#pragma unroll
  for (int s = 0; s < 2; ++s)
#pragma unroll
    for (int qb = 0; qb < 2; ++qb) sts_b128(blockA + (s * 2 + qb) * 1024, pack8(acc[0][qb], s));
  zero16(acc[0][0]); zero16(acc[0][1]); zero16(acc[1][0]); zero16(acc[1][1]);
#pragma unroll
  for (int i = 0; i < 4; ++i) ov.v[i] = r8 * 512 + ctr + i * 128;
  BAR();

#pragma unroll
  for (int j = 0; j < 8; ++j) {
    const int dc = j >> 1, kb = j & 1;
    Frag& f = fr[dc & 1];
    if (kb == 0) {
#pragma unroll
      for (int s = 0; s < 4; ++s)
#pragma unroll
        for (int qb = 0; qb < 2; ++qb)
          f.b[s][qb] = lds_b128(kRegionA + (2 * dc + (s >> 1)) * 8192 + ((s & 1) * 2 + qb) * 1024 + l * 16);
    }
    if (j <= 2) vmwait<8>();
    const unsigned so = ((j + 1) % 3) * 32768;
#pragma unroll
    for (int s = 0; s < 4; ++s) f.a[s] = lds_b128(mine + so + xrow + ((s * 32) ^ xsw));
    SB();
#pragma unroll
    for (int i = 0; i < 8; ++i) {
      acc[kb][i & 1] = mfma(f.a[i >> 1], f.b[i >> 1][i & 1], acc[kb][i & 1]);
      SB();
      if (i == 1 && j >= 5) LGKM0();
      if (i == 1 && j == 3) vmwait<4>();
      if (i == 1 && j == 4) vmwait<0>();
      if (i & 1) {
        const int k = i >> 1;
        if (j <= 4) {
          put(R[j % 3], putb + so, k);
          if (j <= 1) get_piece<kNtKV>(R[j % 3].r[k], k_unit(j + 6), ok.v[k]);
        } else {
          dma_piece<kNtKV>(v_unit(j - 5), mine + so + k * 1024, ov.v[k]);
        }
      }
      SB();
    }
  }

  float lsum[2];
  {
    float mx[2];
#pragma unroll
    for (int qb = 0; qb < 2; ++qb) {
      float m = acc[0][qb][0];
#pragma unroll
      for (int kb = 0; kb < 2; ++kb)
#pragma unroll
        for (int r = 0; r < 16; ++r) m = fmaxf(m, acc[kb][qb][r]);
      mx[qb] = fmaxf(m, other_half(m));
    }
    sts_f1(kRegionA + w * 8192 + 4096 + l * 4, h ? mx[1] : mx[0]);
    get_unit<kNtKV>(R[2], v_unit(3), ov);
    BAR();
    float m = lds_f1(kRegionA + 4096 + l * 4);
#pragma unroll
    for (int i = 1; i < 8; ++i) m = fmaxf(m, lds_f1(kRegionA + i * 8192 + 4096 + l * 4));
    const float mo = other_half(m);
    const float sc = 0.0625f * 1.44269504088896340736f;
    const float m0 = (h ? mo : m) * sc, m1 = (h ? m : mo) * sc;
    BAR();
#pragma unroll
    for (int kb = 0; kb < 2; ++kb) {
#pragma unroll
      for (int qb = 0; qb < 2; ++qb) {
        float sum = kb ? lsum[qb] : 0.f;
        const float mm = qb ? m1 : m0;
#pragma unroll
        for (int r = 0; r < 16; ++r) {
          const float e = __builtin_amdgcn_exp2f(fmaf(acc[kb][qb][r], sc, -mm));
          acc[kb][qb][r] = e;
          sum += e;
        }
        lsum[qb] = sum;
#pragma unroll
        for (int s = 0; s < 2; ++s) sts_b128(blockA + ((2 * kb + s) * 2 + qb) * 1024, pack8(acc[kb][qb], s));
        if (qb == 0) get_unit<kNtKV>(R[kb], v_unit(4 + kb), ov);
      }
    }
    lsum[0] += other_half(lsum[0]);
    lsum[1] += other_half(lsum[1]);
    zero16(acc[0][0]); zero16(acc[0][1]);
  }
#pragma unroll
  for (int i = 0; i < 4; ++i) oo.v[i] = (r8 + 8 * i) * 896 + ctr;

  constexpr unsigned kStat = kRing + 2 * 32768 + 7 * 4096;
  auto read_v = [&](Frag& f, int j, int i) {
    if (i < 4) {
      const unsigned aa = trs + (j % 3) * 32768 + h * 512 + 8 * p + i * 8192;
      f.a[i] = cat(lds_tr(aa), lds_tr(aa + 4096));
    }
    f.b[i >> 1][i & 1] = lds_b128(kRegionA + j * 8192 + i * 1024 + l * 16);
  };
  auto wo_unit = [&](int j) { return wo + ((j + b) & 7) * (32 * 896); };
  auto wo_buf = [&](int j) {
    return (j & 3) == 1 ? mine + 65536 : (j & 3) == 2 ? mine : kRegionA + (unsigned)w * 4096 + ((j & 3) ? 32768 : 0);
  };
  vmwait<20>();
  BAR();
#pragma unroll
  for (int i = 0; i < 8; ++i) read_v(fr[0], 0, i);
  float tot[2][8];
#pragma unroll
  for (int j = 0; j < 8; ++j) {
    if (j == 0) vmwait<16>();
    if (j == 7 || j == 0) BAR();
    else if (j == 6) BARN(4);
    else BARN(2);
    if (j == 5) sts_f1(kStat + w * 256 + l * 4, h ? lsum[1] : lsum[0]);
#pragma unroll
    for (int i = 0; i < 8; ++i) {
      acc[0][i & 1] = mfma(fr[j & 1].a[i >> 1], fr[j & 1].b[i >> 1][i & 1], acc[0][i & 1]);
      SB();
      if (j < 7) read_v(fr[(j + 1) & 1], j + 1, i);
      if (j == 7 && i < 4) {
#pragma unroll
        for (int c = 0; c < 4; ++c)
          tot[i >> 1][(i & 1) * 4 + c] = lds_f1(kStat + ((i & 1) * 4 + c) * 256 + (32 * (i >> 1) + (l & 31)) * 4);
      }
      SB();
      const int k = i - 2;
      Unit& ru = R[(j + 2) % 3];
      if (i == 2 && j <= 2) vmwait<8>();
      if (i == 2 && (j == 3 || j == 4)) {
        if (ow) vmwait<8>();
        else if (j == 3) vmwait<4>();
        else vmwait<0>();
      }
      if (i >= 2 && i < 6 && j <= 4) {
        put(ru, putb + (j % 3) * 32768, k);
        if (j <= 1) get_piece<kNtKV>(ru.r[k], v_unit(j + 6), ov.v[k]);
        else if (ow) get_piece<false>(ru.r[k], wo_unit(j + 2), oo.v[k]);
      }
      if (j == 4 && ow && i >= 6) {
        dma_piece<false>(wo_unit(0), wo_buf(0) + (2 * i - 12) * 1024, oo.v[2 * i - 12]);
        dma_piece<false>(wo_unit(0), wo_buf(0) + (2 * i - 11) * 1024, oo.v[2 * i - 11]);
      }
      if (j >= 5 && ow && !(i & 1)) dma_piece<false>(wo_unit(j - 4), wo_buf(j - 4) + (i >> 1) * 1024, oo.v[i >> 1]);
      SB();
    }
  }
#pragma unroll
  for (int qb = 0; qb < 2; ++qb) {
    float t = 0.f;
#pragma unroll
    for (int i = 0; i < 8; ++i) t += tot[qb][i];
    const float inv = 1.0f / t;
#pragma unroll
    for (int s = 0; s < 2; ++s) sts_b128(mine + 32768 + (s * 2 + qb) * 1024 + l * 16, pack8(acc[0][qb], s, inv));
  }
  zero16(acc[0][0]); zero16(acc[0][1]); zero16(acc[1][0]); zero16(acc[1][1]);
  BAR();

  if (ow) {
    const unsigned tro = trl + 64 * ((q4 >> 1) & 1) + h * 512 + 8 * pp;
    auto read_o = [&](Frag& f, int j, int i) {
      const unsigned src = wo_buf(j) + tro;
      const unsigned a = ((i & 1) ? (src ^ 64u) : src) + (i >> 1) * 2048;
      f.a[i] = cat(lds_tr(a), lds_tr(a + 1024));
      f.b[i >> 1][i & 1] = lds_b128(kRing + 32768 + ((j + b) & 7) * 4096 + i * 1024 + l * 16);
    };
    vmwait<12>();
#pragma unroll
    for (int i = 0; i < 4; ++i) read_o(fr[0], 0, i);
#pragma unroll
    for (int j = 0; j < 8; ++j) {
      if (j <= 1) vmwait<8>();
      if (j == 2) vmwait<4>();
      const Frag& f = fr[j & 1];
      Unit& ru = R[(j + 1) % 3];
#pragma unroll
      for (int i = 0; i < 8; ++i) {
        const int s = i >> 2, cb = (i >> 1) & 1, qb = i & 1;
        acc[cb][qb] = mfma(f.a[2 * s + cb], f.b[s][qb], acc[cb][qb]);
        SB();
        if (j < 7 && i < 4) read_o(fr[(j + 1) & 1], j + 1, i);
        SB();
        if (j == 3 && i == 4) vmwait<0>();
        if (j <= 3 && i >= 4) {
          put(ru, wo_buf(j) + l * 16, i - 4);
          if (j == 0) get_piece<false>(ru.r[i - 4], wo_unit(7), oo.v[i - 4]);
        }
        SB();
      }
    }
    const char* dst = part + (size_t)((br * 8 + b) * kHq + hq) * kPartBytes + w * 8192;
    const unsigned so = (l & 31) * 32 + h * 16;
#pragma unroll
    for (int nb = 0; nb < 2; ++nb)
#pragma unroll
      for (int qb = 0; qb < 2; ++qb)
#pragma unroll
        for (int s = 0; s < 2; ++s)
          asm volatile("global_store_dwordx4 %0, %1, %2 sc1\n\ts_nop 1" ::"v"(so + ((nb * 2 + qb) * 2 + s) * 1024),
                       "v"(pack8(acc[nb][qb], s)), "s"(dst) : "memory");
    VMW(0);
    asm volatile("s_barrier\n\ts_barrier" ::: "memory");

    const char* src = part + (size_t)((br * 8 + b) * kHq) * kPartBytes + w * 8192 + hq * 512;
    const unsigned lo = h * (8 * kPartBytes) + (l & 31) * 16;
    u32x4 v[8];
    asm volatile(
        "global_load_dwordx4 %0, %8, %16 sc1\n\tglobal_load_dwordx4 %1, %9, %16 sc1\n\t"
        "global_load_dwordx4 %2, %10, %16 sc1\n\tglobal_load_dwordx4 %3, %11, %16 sc1\n\t"
        "global_load_dwordx4 %4, %12, %16 sc1\n\tglobal_load_dwordx4 %5, %13, %16 sc1\n\t"
        "global_load_dwordx4 %6, %14, %16 sc1\n\tglobal_load_dwordx4 %7, %15, %16 sc1\n\t"
        "s_waitcnt vmcnt(0)"
        : "=&v"(v[0]), "=&v"(v[1]), "=&v"(v[2]), "=&v"(v[3]), "=&v"(v[4]), "=&v"(v[5]), "=&v"(v[6]), "=&v"(v[7])
        : "v"(lo), "v"(lo + kPartBytes), "v"(lo + 2 * kPartBytes), "v"(lo + 3 * kPartBytes), "v"(lo + 4 * kPartBytes),
          "v"(lo + 5 * kPartBytes), "v"(lo + 6 * kPartBytes), "v"(lo + 7 * kPartBytes), "s"(src)
        : "memory");
    float sum[8];
#pragma unroll
    for (int j = 0; j < 8; ++j) sum[j] = 0.f;
#pragma unroll
    for (int i = 0; i < 8; ++i)
#pragma unroll
      for (int j = 0; j < 4; ++j) {
        sum[2 * j] += __builtin_bit_cast(float, v[i][j] << 16);
        sum[2 * j + 1] += __builtin_bit_cast(float, v[i][j] & 0xffff0000u);
      }
    bf16x4 r;
#pragma unroll
    for (int j = 0; j < 2; ++j) {
      const float k0 = h ? sum[4 + 2 * j] : sum[2 * j], k1 = h ? sum[5 + 2 * j] : sum[1 + 2 * j];
      const float g0 = h ? sum[2 * j] : sum[4 + 2 * j], g1 = h ? sum[1 + 2 * j] : sum[5 + 2 * j];
      f32x2 pr = {k0 + other_half(g0), k1 + other_half(g1)};
      bf16x2 cv = __builtin_convertvector(pr, bf16x2);
      r[2 * j] = cv[0];
      r[2 * j + 1] = cv[1];
    }
    const int fi = hq >> 1, t = l & 31;
    const int q = 32 * ((fi >> 1) & 1) + 16 * (hq & 1) + (t >> 1);
    const int n = 64 * w + 32 * (fi >> 2) + 16 * (fi & 1) + 8 * (t & 1) + 4 * h;
    *(bf16x4*)(out + ((size_t)(b * kSq + q) * (2 * kD) + br * kD + n) * 2) = r;
  } else {
    u64* flags = (u64*)(part + kFlagOff) + (br * 8 + b) * 32;
    const u64 tag = __hip_atomic_load(flags + 16, __ATOMIC_RELAXED, __HIP_MEMORY_SCOPE_AGENT) + 1;
    asm volatile("s_waitcnt vmcnt(0)\n\ts_barrier" ::: "memory");
    if (l == 0) __hip_atomic_store(flags + hq, tag, __ATOMIC_RELAXED, __HIP_MEMORY_SCOPE_AGENT);
    for (unsigned spin = 0; spin < kSpinCap; ++spin) {
      const u64 f = __hip_atomic_load(flags + (l & 15), __ATOMIC_RELAXED, __HIP_MEMORY_SCOPE_AGENT);
      if (__all(f == tag)) break;
      __builtin_amdgcn_s_sleep(1);
    }
    asm volatile("s_barrier" ::: "memory");
    if (hq == 0 && l == 0) __hip_atomic_store(flags + 16, tag, __ATOMIC_RELAXED, __HIP_MEMORY_SCOPE_AGENT);
  }
}

extern "C" void kernel_launch(void* const* d_in, const int* in_sizes, int n_in, void* d_out, int out_size, void* d_ws,
                              size_t ws_size, hipStream_t stream) {
  (void)in_sizes; (void)n_in; (void)out_size; (void)ws_size;
  (void)hipFuncSetAttribute((const void*)gqa_fused_kernel, hipFuncAttributeMaxDynamicSharedMemorySize, kLdsBytes);
  gqa_fused_kernel<<<256, 512, kLdsBytes, stream>>>(
      (const char*)d_in[0], (const char*)d_in[1], (const char*)d_in[2], (const char*)d_in[3], (const char*)d_in[4],
      (const char*)d_in[5], (const char*)d_in[6], (const char*)d_in[7], (const char*)d_in[8], (const char*)d_in[9],
      (char*)d_ws, (char*)d_out);
}
```
